```python
import torch

def _softmax(x, axis=-1):
    return torch.softmax(x, dim=axis)
def _gelu(x):
    return torch.nn.functional.gelu(x, approximate="tanh")
def _silu(x):
    return torch.nn.functional.silu(x)
def _sigmoid(x):
    return torch.sigmoid(x)
def _layernorm(x, axis=-1, eps=1e-5):
    m = x.mean(dim=axis, keepdim=True)
    v = x.var(dim=axis, keepdim=True, unbiased=False)
    return (x - m) / torch.sqrt(v + eps)
def _scatter_add(tgt, idx, src, axis=0):
    return tgt.clone().index_add_(axis, idx, src)
def _segment_sum(src, seg, n):
    out = torch.zeros((n,) + src.shape[1:], dtype=src.dtype)
    return out.index_add_(0, seg, src)
def _pad(x, pw):
    flat = tuple(v for pair in reversed(pw) for v in pair)
    return torch.nn.functional.pad(x, flat)

kernel_name = 'gated_mix__mha_block_cached__glu_gated_bilinear__decode'

def setup_inputs():

    _w = (lambda a: (lambda t: t.to(torch.bfloat16) if t.is_floating_point() else t)(torch.as_tensor(a)))
    def _A_setup():
        B, Sq, D = 4, 1, 640
        H, Sk, Dh = 32, 4096, 128
        return {
            "x": _w((lambda *s: torch.randn(*s, dtype=torch.bfloat16) * 0.05)(B, Sq, D)),
            "k_cache": _w((lambda *s: torch.randn(*s, dtype=torch.bfloat16) * 6.4)(B, H, Sk, Dh)),
            "v_cache": _w((lambda *s: torch.randn(*s, dtype=torch.bfloat16) * 0.05)(B, H, Sk, Dh)),
            "wq": _w((lambda *s: torch.randn(*s, dtype=torch.bfloat16) * 6.4)(D, H * Dh) * (D ** -0.5)),
            "wo": _w((lambda *s: torch.randn(*s, dtype=torch.bfloat16) * 0.05)(H * Dh, D) * ((H * Dh) ** -0.5)),
        }
    _A_in = _A_setup()

    def _B_setup():
        B, S, D, F = 4, 1, 640, 1728
        return {
            "x": _w((lambda *s: torch.randn(*s, dtype=torch.bfloat16) * 0.05)(B, S, D)),
            "w_gate": _w((lambda *s: torch.randn(*s, dtype=torch.bfloat16) * 0.4)(D, F) * (D ** -0.5)),
            "w_up": _w((lambda *s: torch.randn(*s, dtype=torch.bfloat16) * 0.4)(D, F) * (D ** -0.5)),
            "w_down": _w((lambda *s: torch.randn(*s, dtype=torch.bfloat16) * 0.4)(F, D) * (F ** -0.5)),
        }
    _B_in = _B_setup()

    return {
        "A_x": _A_in["x"],
        "A_k_cache": _A_in["k_cache"],
        "A_v_cache": _A_in["v_cache"],
        "A_wq": _A_in["wq"],
        "A_wo": _A_in["wo"],
        "B_x": _B_in["x"],
        "B_w_gate": _B_in["w_gate"],
        "B_w_up": _B_in["w_up"],
        "B_w_down": _B_in["w_down"],
        "gate_w": _w((lambda *s: torch.randn(*s, dtype=torch.bfloat16) * 0.05)(640) * 16.0),
    }

def reference(A_x, A_k_cache, A_v_cache, A_wq, A_wo, B_x, B_w_gate, B_w_up, B_w_down, gate_w):
    def _A_ref(x, k_cache, v_cache, wq, wo):
        B, Sq, D = x.shape
        H, Dh = 32, 128
        q = (lambda x, *d: x.permute(*d))((x @ wq).reshape(B, Sq, H, Dh), 0, 2, 1, 3)
        scores = torch.einsum("bhqd,bhkd->bhqk", q, k_cache) * (Dh ** -0.5)
        p = _softmax(scores, axis=-1)
        o = torch.einsum("bhqk,bhkd->bhqd", p, v_cache)
        return (lambda x, *d: x.permute(*d))(o, 0, 2, 1, 3).reshape(B, Sq, H * Dh) @ wo

    def _B_ref(x, w_gate, w_up, w_down):
        g = torch.einsum("bsd,df->bsf", x, w_gate)
        u = torch.einsum("bsd,df->bsf", x, w_up)
        h = g * u
        return torch.einsum("bsf,fd->bsd", h, w_down)

    x = A_x
    g = _sigmoid((x * gate_w).sum(-1, keepdims=True))
    a = _A_ref(x, A_k_cache, A_v_cache, A_wq, A_wo)
    b = _B_ref(x, B_w_gate, B_w_up, B_w_down)
    return g * a + (1 - g) * b
```

```cpp
#include <hip/hip_runtime.h>
#include <hip/hip_bf16.h>
#include <stdint.h>

constexpr int B = 4, D = 640, H = 32, SK = 4096, DH = 128, F = 1728, HD = H * DH;
constexpr int NSPLIT = 4;

typedef unsigned u32x4 __attribute__((ext_vector_type(4)));
typedef float f32x4 __attribute__((ext_vector_type(4)));
typedef __bf16 bf16x2 __attribute__((ext_vector_type(2)));

__device__ __forceinline__ float bf2f(unsigned short b) { return __builtin_bit_cast(float, (unsigned)b << 16); }
__device__ __forceinline__ float bflo(unsigned u) { return __builtin_bit_cast(float, u << 16); }
__device__ __forceinline__ float bfhi(unsigned u) { return __builtin_bit_cast(float, u & 0xffff0000u); }
__device__ __forceinline__ unsigned short f2bf(float f) { __bf16 h = (__bf16)f; return __builtin_bit_cast(unsigned short, h); }
__device__ __forceinline__ float rbf(float f) { return bf2f(f2bf(f)); }
__device__ __forceinline__ float dot2(unsigned k, unsigned q, float acc) {
  return __builtin_amdgcn_fdot2_f32_bf16(__builtin_bit_cast(bf16x2, k), __builtin_bit_cast(bf16x2, q), acc, false);
}
template <int CTRL> __device__ __forceinline__ float dpp(float x) {
  return __builtin_bit_cast(float, __builtin_amdgcn_mov_dpp(__builtin_bit_cast(int, x), CTRL, 0xf, 0xf, true));
}
constexpr int XOR1 = 0xB1, XOR2 = 0x4E, XOR7 = 0x141, XOR8 = 0x128, ROR4 = 0x124;

constexpr size_t WS_Q = 0, WS_HH = 65536, WS_ML = 131072, WS_ACC = 262144;

__global__ void __launch_bounds__(256) k_proj(const unsigned short* x, const unsigned short* wq, const unsigned short* wg, const unsigned short* wu, unsigned char* ws) {
  __shared__ float xs[B][D];
  __shared__ float red[4][2][B][64];
  const int tid = threadIdx.x, lane = tid & 63, w = tid >> 6;
  for (int i = tid; i < B * D; i += 256) xs[i / D][i % D] = bf2f(x[i]);
  __syncthreads();
  const int blk = blockIdx.x;
  if (blk < HD / 64) {
    const int col = blk * 64 + lane;
    float a[B] = {0, 0, 0, 0};
    for (int k = w * 160; k < w * 160 + 160; ++k) { float wv = bf2f(wq[(size_t)k * HD + col]);
      for (int b = 0; b < B; ++b) a[b] = fmaf(xs[b][k], wv, a[b]); }
    for (int b = 0; b < B; ++b) red[w][0][b][lane] = a[b];
    __syncthreads();
    if (w < B) { float s = red[0][0][w][lane] + red[1][0][w][lane] + red[2][0][w][lane] + red[3][0][w][lane];
      ((unsigned short*)(ws + WS_Q))[w * HD + col] = f2bf(s); }
  } else {
    const int col = (blk - HD / 64) * 64 + lane;
    float a[B] = {0, 0, 0, 0}, u[B] = {0, 0, 0, 0};
    for (int k = w * 160; k < w * 160 + 160; ++k) { float g = bf2f(wg[(size_t)k * F + col]), uu = bf2f(wu[(size_t)k * F + col]);
      for (int b = 0; b < B; ++b) { a[b] = fmaf(xs[b][k], g, a[b]); u[b] = fmaf(xs[b][k], uu, u[b]); } }
    for (int b = 0; b < B; ++b) { red[w][0][b][lane] = a[b]; red[w][1][b][lane] = u[b]; }
    __syncthreads();
    if (w < B) { float s = red[0][0][w][lane] + red[1][0][w][lane] + red[2][0][w][lane] + red[3][0][w][lane];
      float t = red[0][1][w][lane] + red[1][1][w][lane] + red[2][1][w][lane] + red[3][1][w][lane];
      ((float*)(ws + WS_HH))[w * F + col] = rbf(rbf(s) * rbf(t)); }
  }
}

__global__ void __launch_bounds__(256) k_attn(const unsigned short* kc, const unsigned short* vc, unsigned char* ws) {
  __shared__ float mrg[16][132];
  const int tid = threadIdx.x, lane = tid & 63, w = __builtin_amdgcn_readfirstlane(tid >> 6), r = lane >> 4, c = lane & 15;
  const int bh = blockIdx.x / NSPLIT, s = blockIdx.x % NSPLIT;
  constexpr int KPW = SK / NSPLIT / 4;
  constexpr float C2 = 0.088388347648318440f * 1.4426950408889634f;
  const u32x4 q = *(const u32x4*)((const unsigned short*)(ws + WS_Q) + bh * DH + 8 * c);
  const size_t base = ((size_t)bh * SK + s * (SK / NSPLIT) + w * KPW) * DH + lane * 8;
  const u32x4* kp = (const u32x4*)(kc + base);
  const u32x4* vp = (const u32x4*)(vc + base);
  float m = -INFINITY, l = 0.f, acc[8] = {0, 0, 0, 0, 0, 0, 0, 0};
  for (int t = 0; t < KPW / 64; ++t) {
    u32x4 K[16], V[16];
#pragma unroll
    for (int i = 0; i < 16; ++i) K[i] = __builtin_nontemporal_load(kp + (t * 16 + i) * 64);
#pragma unroll
    for (int i = 0; i < 16; ++i) V[i] = __builtin_nontemporal_load(vp + (t * 16 + i) * 64);
    float x[16];
#pragma unroll
    for (int i = 0; i < 16; ++i) { float d = 0.f;
#pragma unroll
      for (int j = 0; j < 4; ++j) d = dot2(K[i][j], q[j], d);
      x[i] = d; }
#define TR_STEP(HALF, CTRL, BIT) _Pragma("unroll") for (int h = 0; h < (HALF); ++h) { \
      const float lo_ = x[h], hi_ = x[h + (HALF)]; const float t1_ = lo_ + dpp<CTRL>(lo_), t2_ = hi_ + dpp<CTRL>(hi_); \
      x[h] = (c & (BIT)) ? t2_ : t1_; }
    TR_STEP(8, XOR8, 8) TR_STEP(4, XOR7, 4) TR_STEP(2, XOR2, 2) TR_STEP(1, XOR1, 1)
#undef TR_STEP
    const float sc = rbf(rbf(x[0]) * 0.088388347648318440f) * 1.4426950408889634f;
    float pm = sc; pm = fmaxf(pm, dpp<XOR1>(pm)); pm = fmaxf(pm, dpp<XOR2>(pm)); pm = fmaxf(pm, dpp<XOR7>(pm)); pm = fmaxf(pm, dpp<XOR8>(pm));
    const float mn = fmaxf(m, pm);
    const float alpha = __builtin_amdgcn_exp2f(m - mn), p = __builtin_amdgcn_exp2f(sc - mn);
    l = fmaf(l, alpha, p); m = mn;
#pragma unroll
    for (int k = 0; k < 8; ++k) acc[k] *= alpha;
#define PV(i) { const float pi = dpp<0x150 + (i)>(p); \
      _Pragma("unroll") for (int j = 0; j < 4; ++j) { acc[2 * j] = fmaf(pi, bflo(V[i][j]), acc[2 * j]); acc[2 * j + 1] = fmaf(pi, bfhi(V[i][j]), acc[2 * j + 1]); } }
    PV(0) PV(1) PV(2) PV(3) PV(4) PV(5) PV(6) PV(7) PV(8) PV(9) PV(10) PV(11) PV(12) PV(13) PV(14) PV(15)
#undef PV
  }
  l += dpp<XOR1>(l); l += dpp<XOR2>(l); l += dpp<XOR7>(l); l += dpp<XOR8>(l);
  float* rec = mrg[w * 4 + r];
#pragma unroll
  for (int k = 0; k < 8; ++k) rec[4 + 8 * c + k] = acc[k];
  if (c == 0) { rec[0] = m; rec[1] = l; }
  __syncthreads();
  if (tid < 128) {
    float M = -INFINITY;
    for (int i = 0; i < 16; ++i) M = fmaxf(M, mrg[i][0]);
    float L = 0.f, o = 0.f;
    for (int i = 0; i < 16; ++i) { const float wt = __builtin_amdgcn_exp2f(mrg[i][0] - M); L = fmaf(wt, mrg[i][1], L); o = fmaf(wt, mrg[i][4 + tid], o); }
    ((float*)(ws + WS_ACC))[(size_t)blockIdx.x * DH + tid] = o;
    if (tid == 0) { ((float*)(ws + WS_ML))[blockIdx.x * 2] = M; ((float*)(ws + WS_ML))[blockIdx.x * 2 + 1] = L; }
  }
}

__global__ void __launch_bounds__(256) k_out(const unsigned short* x, const unsigned short* gw, const unsigned short* wo, const unsigned short* wd, const unsigned char* ws, unsigned short* out) {
  __shared__ float o[B][HD];
  __shared__ float red[16][2][B][16];
  __shared__ float gs[B];
  const int tid = threadIdx.x;
  const float* ml = (const float*)(ws + WS_ML);
  const float* ac = (const float*)(ws + WS_ACC);
  for (int i = tid; i < B * HD; i += 256) {
    const int bh = i / DH, d = i % DH;
    float M = -INFINITY;
    for (int s = 0; s < NSPLIT; ++s) M = fmaxf(M, ml[(bh * NSPLIT + s) * 2]);
    float L = 0.f, v = 0.f;
    for (int s = 0; s < NSPLIT; ++s) { const float wt = __builtin_amdgcn_exp2f(ml[(bh * NSPLIT + s) * 2] - M); L = fmaf(wt, ml[(bh * NSPLIT + s) * 2 + 1], L); v = fmaf(wt, ac[(size_t)(bh * NSPLIT + s) * DH + d], v); }
    o[i / HD][i % HD] = v / L;
  }
  if (tid < 64 * B) { const int b = tid >> 6, lane = tid & 63; float s = 0.f;
    for (int k = lane; k < D; k += 64) s += rbf(bf2f(x[b * D + k]) * bf2f(gw[k]));
    for (int off = 32; off; off >>= 1) s += __shfl_xor(s, off);
    if (lane == 0) { s = rbf(s); gs[b] = rbf(1.f / (1.f + __expf(-s))); } }
  __syncthreads();
  const int cc = tid & 15, kg = tid >> 4, col = blockIdx.x * 16 + cc;
  float a[B] = {0, 0, 0, 0}, f[B] = {0, 0, 0, 0};
  for (int k = kg; k < HD; k += 16) { const float wv = bf2f(wo[(size_t)k * D + col]);
    for (int b = 0; b < B; ++b) a[b] = fmaf(o[b][k], wv, a[b]); }
  const float* hh = (const float*)(ws + WS_HH);
  for (int k = kg; k < F; k += 16) { const float wv = bf2f(wd[(size_t)k * D + col]);
    for (int b = 0; b < B; ++b) f[b] = fmaf(hh[b * F + k], wv, f[b]); }
  for (int b = 0; b < B; ++b) { red[kg][0][b][cc] = a[b]; red[kg][1][b][cc] = f[b]; }
  __syncthreads();
  if (tid < 64) { const int b = tid >> 4; float sa = 0.f, sf = 0.f;
    for (int i = 0; i < 16; ++i) { sa += red[i][0][b][cc]; sf += red[i][1][b][cc]; }
    const float g = gs[b];
    out[b * D + col] = f2bf(g * sa + (1.f - g) * sf);
  }
}

extern "C" void kernel_launch(void* const* d_in, const int* in_sizes, int n_in, void* d_out, int out_size, void* d_ws, size_t ws_size, hipStream_t stream) {
  const unsigned short* x = (const unsigned short*)d_in[0];
  const unsigned short* kc = (const unsigned short*)d_in[1];
  const unsigned short* vc = (const unsigned short*)d_in[2];
  const unsigned short* wq = (const unsigned short*)d_in[3];
  const unsigned short* wo = (const unsigned short*)d_in[4];
  const unsigned short* wg = (const unsigned short*)d_in[6];
  const unsigned short* wu = (const unsigned short*)d_in[7];
  const unsigned short* wd = (const unsigned short*)d_in[8];
  const unsigned short* gw = (const unsigned short*)d_in[9];
  unsigned char* ws = (unsigned char*)d_ws;
  k_proj<<<HD / 64 + F / 64, 256, 0, stream>>>(x, wq, wg, wu, ws);
  k_attn<<<B * H * NSPLIT, 256, 0, stream>>>(kc, vc, ws);
  k_out<<<D / 16, 256, 0, stream>>>(x, gw, wo, wd, ws, (unsigned short*)d_out);
}
```

```cpp
#include <hip/hip_runtime.h>
#include <hip/hip_bf16.h>
#include <stdint.h>

namespace gm {
constexpr int B = 4, D = 640, H = 32, SK = 4096, DH = 128, F = 1728, HD = H * DH;
constexpr int NSV = 5, NSW = 4, NTHR = 64 * (NSV + NSW);
#define GM_DEPTH 4
constexpr int DEPTH = GM_DEPTH;
constexpr int NPIECE = (SK / 2) / 16 / NSW;
static_assert(NPIECE % DEPTH == 0, "ring");

typedef unsigned u32x4 __attribute__((ext_vector_type(4)));
typedef unsigned u32x2 __attribute__((ext_vector_type(2)));
typedef float f32x4 __attribute__((ext_vector_type(4)));
typedef float f32x2 __attribute__((ext_vector_type(2)));
typedef __bf16 bf16x2 __attribute__((ext_vector_type(2)));
typedef unsigned short u16;
#define GAS __attribute__((address_space(1)))

constexpr int WS_EPOCH = 0;
constexpr int WS_R1 = 4096;
constexpr int R1_REC = 96, R2_REC = 80;
constexpr int WS_R2 = WS_R1 + B * 64 * 64 * R1_REC;
constexpr int WS_END = WS_R2 + B * 64 * 64 * R2_REC;
#define STAMP(i) do { } while (0)

constexpr int L_QP = 0;
constexpr int L_FP = L_QP + 5 * 128;
constexpr int L_REC = L_FP + 5 * 64;
constexpr int L_M = L_REC + 4 * 128;
constexpr int L_L = L_M + 4;
constexpr int L_TAG = L_L + 4;
constexpr int L_G = L_TAG + 4;
constexpr int L_END = L_G + (384 + 320) * 4;

__device__ __forceinline__ float bflo(unsigned u) { return __builtin_bit_cast(float, u << 16); }
__device__ __forceinline__ float bfhi(unsigned u) { return __builtin_bit_cast(float, u & 0xffff0000u); }
__device__ __forceinline__ unsigned pk2(float lo, float hi) { bf16x2 v = {(__bf16)lo, (__bf16)hi}; return __builtin_bit_cast(unsigned, v); }
__device__ __forceinline__ float rbf(float f) { return bflo(pk2(f, f)); }
__device__ __forceinline__ float dot2(unsigned a, unsigned b, float acc) {
  return __builtin_amdgcn_fdot2_f32_bf16(__builtin_bit_cast(bf16x2, a), __builtin_bit_cast(bf16x2, b), acc, false);
}
template <int CTRL> __device__ __forceinline__ float dpp(float x) {
  return __builtin_bit_cast(float, __builtin_amdgcn_mov_dpp(__builtin_bit_cast(int, x), CTRL, 0xf, 0xf, true));
}
template <int CTRL> __device__ __forceinline__ unsigned dppu(unsigned x) {
  return (unsigned)__builtin_amdgcn_mov_dpp((int)x, CTRL, 0xf, 0xf, true);
}
constexpr int XOR1 = 0xB1, XOR2 = 0x4E, XOR7 = 0x141, ROR8 = 0x128, ROR4 = 0x124, BCAST = 0x150;
__device__ __forceinline__ float tr32(float a, float b) {
  auto t = __builtin_amdgcn_permlane32_swap(__float_as_uint(a), __float_as_uint(b), false, false);
  return __uint_as_float(t[0]) + __uint_as_float(t[1]);
}
__device__ __forceinline__ float tr16(float a, float b) {
  auto t = __builtin_amdgcn_permlane16_swap(__float_as_uint(a), __float_as_uint(b), false, false);
  return __uint_as_float(t[0]) + __uint_as_float(t[1]);
}
__device__ __forceinline__ float xrow_max(float x) {
  auto s = __builtin_amdgcn_permlane16_swap(__float_as_uint(x), __float_as_uint(x), false, false);
  x = fmaxf(__uint_as_float(s[0]), __uint_as_float(s[1]));
  auto t = __builtin_amdgcn_permlane32_swap(__float_as_uint(x), __float_as_uint(x), false, false);
  return fmaxf(__uint_as_float(t[0]), __uint_as_float(t[1]));
}
__device__ __forceinline__ float xrow_sum(float x) { x = tr16(x, x); return tr32(x, x); }
typedef __amdgpu_buffer_rsrc_t rsrc_t;
__device__ __forceinline__ rsrc_t mkrs(const void* p, int bytes) { return __builtin_amdgcn_make_buffer_rsrc((void*)p, 0, bytes, 0x00020000); }
#define GM_WAUX 0
template <int AUX> __device__ __forceinline__ u32x4 ldb(rsrc_t r, int voff, int soff) { return __builtin_amdgcn_raw_buffer_load_b128(r, voff, soff, AUX); }
__device__ __forceinline__ u32x4 ldw(const void* p) { return *(const GAS u32x4*)p; }
__device__ __forceinline__ unsigned plo(unsigned r0, unsigned r1) { return __builtin_amdgcn_perm(r1, r0, 0x05040100u); }
__device__ __forceinline__ unsigned phi(unsigned r0, unsigned r1) { return __builtin_amdgcn_perm(r1, r0, 0x07060302u); }
__device__ __forceinline__ void pairup(u32x4& r0, u32x4& r1) {
  u32x4 a, b;
  a[0] = plo(r0[0], r1[0]); a[1] = phi(r0[0], r1[0]); a[2] = plo(r0[1], r1[1]); a[3] = phi(r0[1], r1[1]);
  b[0] = plo(r0[2], r1[2]); b[1] = phi(r0[2], r1[2]); b[2] = plo(r0[3], r1[3]); b[3] = phi(r0[3], r1[3]);
  r0 = a; r1 = b;
}
__device__ __forceinline__ void dot8(const u32x4& a, const u32x4& b, unsigned xp, float (&acc)[8]) {
#pragma unroll
  for (int j = 0; j < 4; ++j) { acc[j] = dot2(a[j], xp, acc[j]); acc[4 + j] = dot2(b[j], xp, acc[4 + j]); }
}
template <int K, int N, bool PAIR> struct Gemv {
  static __device__ __forceinline__ void run(u32x4 (&W)[32], unsigned vp, float (&acc)[8]) {
    const unsigned xp = dppu<BCAST + K>(vp);
    if (PAIR) pairup(W[2 * K], W[2 * K + 1]);
    dot8(W[2 * K], W[2 * K + 1], xp, acc);
    if constexpr (K + 1 < N) Gemv<K + 1, N, PAIR>::run(W, vp, acc);
  }
};
__device__ __forceinline__ f32x2 red_rows(const float (&a)[8]) {
  const float r0 = tr32(a[0], a[4]), r1 = tr32(a[1], a[5]), r2 = tr32(a[2], a[6]), r3 = tr32(a[3], a[7]);
  return f32x2{tr16(r0, r2), tr16(r1, r3)};
}
#define LGKM0() asm volatile("s_waitcnt lgkmcnt(0)" ::: "memory")
#define BAR() do { LGKM0(); __builtin_amdgcn_s_barrier(); asm volatile("" ::: "memory"); } while (0)
#define SB() __builtin_amdgcn_sched_barrier(0)

struct Params {
  const u16 *x, *kc, *vc, *wq, *wo, *wg, *wu, *wd, *gw;
  u16* out;
  unsigned char* ws;
};

__global__ void __launch_bounds__(NTHR, 3) gm_fused(Params p) {
  __shared__ __attribute__((aligned(16))) float lds[L_END];
  const int tid = threadIdx.x, lane = tid & 63, wv = __builtin_amdgcn_readfirstlane(tid >> 6);
  const int bid = blockIdx.x, rr = bid >> 3;
  const int h = (bid & 7) + 8 * (rr >> 3), b = (rr >> 1) & 3, s = rr & 1;
  const int g = lane >> 4, c = lane & 15;

  if (wv >= NSV) {
    const int sw = wv - NSV;
    const size_t base = ((size_t)(b * H + h) * SK + s * (SK / 2)) * DH;
    const rsrc_t rk = mkrs(p.kc + base, (SK / 2) * DH * 2), rv = mkrs(p.vc + base, (SK / 2) * DH * 2);
    const int lo = sw * 4096 + lane * 16;
    constexpr int PSTR = NSW * 4096;
    u32x4 K[DEPTH][4], V[DEPTH][4];
#define LDK(d, t) { _Pragma("unroll") for (int i = 0; i < 4; ++i) K[d][i] = ldb<2>(rk, lo + i * 1024, (t) * PSTR); SB(); }
#define LDV(d, t) { _Pragma("unroll") for (int i = 0; i < 4; ++i) V[d][i] = ldb<2>(rv, lo + i * 1024, (t) * PSTR); SB(); }
    if (sw == 0) STAMP(0);
#pragma unroll
    for (int d = 0; d < DEPTH; ++d) { LDK(d, d) LDV(d, d) }
    BAR();
    unsigned q[4];
    {
      f32x4 a0 = {0.f, 0.f, 0.f, 0.f}, a1 = a0;
#pragma unroll
      for (int v = 0; v < NSV; ++v) { a0 += *(const f32x4*)&lds[L_QP + v * 128 + 8 * c]; a1 += *(const f32x4*)&lds[L_QP + v * 128 + 8 * c + 4]; }
      q[0] = pk2(a0[0], a0[1]); q[1] = pk2(a0[2], a0[3]); q[2] = pk2(a1[0], a1[1]); q[3] = pk2(a1[2], a1[3]);
    }
    if (sw == 0) STAMP(1);
    float m = -INFINITY, l = 0.f, acc[8] = {0.f, 0.f, 0.f, 0.f, 0.f, 0.f, 0.f, 0.f};
    const bool b8 = (c & 8) != 0, b4 = (c & 4) != 0;
#define STEP(d, LOADK, LOADV) { \
      float x_[4]; \
      _Pragma("unroll") for (int i = 0; i < 4; ++i) { float t_ = dot2(K[d][i][0], q[0], 0.f); t_ = dot2(K[d][i][1], q[1], t_); t_ = dot2(K[d][i][2], q[2], t_); x_[i] = dot2(K[d][i][3], q[3], t_); } \
      asm volatile("" : "+v"(x_[0]), "+v"(x_[1]), "+v"(x_[2]), "+v"(x_[3]) :: "memory"); SB(); LOADK \
      const float y0_ = (b8 ? x_[2] : x_[0]) + dpp<ROR8>(b8 ? x_[0] : x_[2]); \
      const float y1_ = (b8 ? x_[3] : x_[1]) + dpp<ROR8>(b8 ? x_[1] : x_[3]); \
      float z_ = (b4 ? y1_ : y0_) + dpp<XOR7>(b4 ? y0_ : y1_); \
      z_ += dpp<XOR2>(z_); z_ += dpp<XOR1>(z_); \
      const float sc_ = rbf(rbf(z_) * 0.088388347648318440f) * 1.4426950408889634f; \
      float pm_ = fmaxf(sc_, dpp<ROR4>(sc_)); pm_ = fmaxf(pm_, dpp<ROR8>(pm_)); \
      const float mn_ = fmaxf(m, pm_); \
      const float al_ = __builtin_amdgcn_exp2f(m - mn_), pp_ = __builtin_amdgcn_exp2f(sc_ - mn_); \
      l = fmaf(l, al_, pp_); m = mn_; \
      _Pragma("unroll") for (int k = 0; k < 8; ++k) acc[k] *= al_; \
      const float p0_ = dpp<BCAST + 0>(pp_), p1_ = dpp<BCAST + 4>(pp_), p2_ = dpp<BCAST + 8>(pp_), p3_ = dpp<BCAST + 12>(pp_); \
      _Pragma("unroll") for (int j = 0; j < 4; ++j) { acc[2 * j] = fmaf(p0_, bflo(V[d][0][j]), acc[2 * j]); acc[2 * j + 1] = fmaf(p0_, bfhi(V[d][0][j]), acc[2 * j + 1]); } \
      _Pragma("unroll") for (int j = 0; j < 4; ++j) { acc[2 * j] = fmaf(p1_, bflo(V[d][1][j]), acc[2 * j]); acc[2 * j + 1] = fmaf(p1_, bfhi(V[d][1][j]), acc[2 * j + 1]); } \
      _Pragma("unroll") for (int j = 0; j < 4; ++j) { acc[2 * j] = fmaf(p2_, bflo(V[d][2][j]), acc[2 * j]); acc[2 * j + 1] = fmaf(p2_, bfhi(V[d][2][j]), acc[2 * j + 1]); } \
      _Pragma("unroll") for (int j = 0; j < 4; ++j) { acc[2 * j] = fmaf(p3_, bflo(V[d][3][j]), acc[2 * j]); acc[2 * j + 1] = fmaf(p3_, bfhi(V[d][3][j]), acc[2 * j + 1]); } \
      asm volatile("" : "+v"(acc[0]), "+v"(acc[1]), "+v"(acc[2]), "+v"(acc[3]), "+v"(acc[4]), "+v"(acc[5]), "+v"(acc[6]), "+v"(acc[7]) :: "memory"); SB(); LOADV }
#pragma unroll 1
    for (int t = DEPTH; t < NPIECE; t += DEPTH) {
#pragma unroll
      for (int d = 0; d < DEPTH; ++d) STEP(d, LDK(d, t + d), LDV(d, t + d))
    }
#pragma unroll
    for (int d = 0; d < DEPTH; ++d) STEP(d, , )
    if (sw == 0) STAMP(2);
    const float mw = xrow_max(m);
    const float f = __builtin_amdgcn_exp2f(m - mw);
#pragma unroll
    for (int k = 0; k < 8; ++k) acc[k] *= f;
    l *= f; l += dpp<ROR4>(l); l += dpp<ROR8>(l); l = xrow_sum(l);
    const f32x2 o2 = red_rows(acc);
    *(f32x2*)&lds[L_REC + sw * 128 + 8 * c + 2 * g] = o2;
    if (lane == 0) { lds[L_M + sw] = mw; lds[L_L + sw] = l; }
    BAR();
    return;
  }

  const int v = wv;
  const rsrc_t rs = mkrs(p.ws, WS_END);
  const int pj = 2 * h + s;
  u32x4 W[32];
  const unsigned ep = __hip_atomic_load((const GAS unsigned*)(p.ws + WS_EPOCH + b * 256), __ATOMIC_RELAXED, __HIP_MEMORY_SCOPE_AGENT);
  const unsigned xp0 = *(const GAS unsigned*)(p.x + b * D + 128 * v + 2 * lane);
  {
    const rsrc_t rw = mkrs(p.wq, D * HD * 2);
    const int vo = ((128 * v + 32 * g) * HD + h * DH + 8 * c) * 2;
#pragma unroll
    for (int i = 0; i < 32; ++i) { W[i] = ldb<GM_WAUX>(rw, vo, i * (HD * 2)); SB(); }
    SB();
  }
  if (tid == 0) ((unsigned*)lds)[L_TAG + 1] = 0u;
  {
    float acc[8] = {0.f, 0.f, 0.f, 0.f, 0.f, 0.f, 0.f, 0.f};
    Gemv<0, 16, true>::run(W, xp0, acc);
    *(f32x2*)&lds[L_QP + v * 128 + 8 * c + 2 * g] = red_rows(acc);
  }
  if (tid == 0) ((unsigned*)lds)[L_TAG] = ep + 1u;
  BAR();
  const unsigned tag = ((const unsigned*)lds)[L_TAG];

  {
    const int nch = pj < 24 ? 4 : 3, ch0 = pj < 24 ? 4 * pj : 3 * pj + 24;
    const int r16 = lane >> 2, ch = min(lane & 3, nch - 1);
    const int go = ((128 * v + 8 * r16) * F + 8 * (ch0 + ch)) * 2;
    const rsrc_t rg = mkrs(p.wg, D * F * 2), ru = mkrs(p.wu, D * F * 2), rd = mkrs(p.wd, D * F * 2);
    const u32x4 xq = ldw(p.x + b * D + 128 * v + 8 * r16);
#pragma unroll
    for (int i = 0; i < 8; ++i) { W[i] = ldb<GM_WAUX>(rg, go, i * (F * 2)); W[8 + i] = ldb<GM_WAUX>(ru, go, i * (F * 2)); }
#pragma unroll
    for (int i = 0; i < 8; ++i) W[16 + i] = ldb<GM_WAUX>(rd, ((8 * ch0 + 8 * g) * D + 128 * v + 8 * c) * 2, i * (D * 2));
    SB();
    float ag[8] = {0.f, 0.f, 0.f, 0.f, 0.f, 0.f, 0.f, 0.f}, au[8] = {0.f, 0.f, 0.f, 0.f, 0.f, 0.f, 0.f, 0.f};
#pragma unroll
    for (int k = 0; k < 4; ++k) {
      pairup(W[2 * k], W[2 * k + 1]); dot8(W[2 * k], W[2 * k + 1], xq[k], ag);
      pairup(W[8 + 2 * k], W[9 + 2 * k]); dot8(W[8 + 2 * k], W[9 + 2 * k], xq[k], au);
    }
    float t8[8], t4[4];
#pragma unroll
    for (int k = 0; k < 8; ++k) t8[k] = tr32(ag[k], au[k]);
#pragma unroll
    for (int k = 0; k < 4; ++k) { t4[k] = tr16(t8[k], t8[4 + k]); t4[k] += dpp<ROR8>(t4[k]); t4[k] += dpp<ROR4>(t4[k]); }
    if (c < 4) *(f32x4*)&lds[L_FP + v * 64 + (g >> 1) * 32 + c * 8 + 4 * (g & 1)] = f32x4{t4[0], t4[1], t4[2], t4[3]};
    LGKM0();
    if (lane == 0) __hip_atomic_fetch_add((unsigned*)lds + L_TAG + 1, 1u, __ATOMIC_RELAXED, __HIP_MEMORY_SCOPE_WORKGROUP);
    while (__hip_atomic_load((unsigned*)lds + L_TAG + 1, __ATOMIC_RELAXED, __HIP_MEMORY_SCOPE_WORKGROUP) < (unsigned)NSV) __builtin_amdgcn_s_sleep(1);
    asm volatile("" ::: "memory");
    const int fl = 8 * g + 2 * (c & 3);
    f32x2 gs = {0.f, 0.f}, us = {0.f, 0.f};
#pragma unroll
    for (int vv = 0; vv < NSV; ++vv) { gs += *(const f32x2*)&lds[L_FP + vv * 64 + fl]; us += *(const f32x2*)&lds[L_FP + vv * 64 + 32 + fl]; }
    const unsigned hp = g < nch ? pk2(rbf(gs[0]) * rbf(us[0]), rbf(gs[1]) * rbf(us[1])) : 0u;
    float acc[8] = {0.f, 0.f, 0.f, 0.f, 0.f, 0.f, 0.f, 0.f};
    pairup(W[16], W[17]); dot8(W[16], W[17], dppu<BCAST + 0>(hp), acc);
    pairup(W[18], W[19]); dot8(W[18], W[19], dppu<BCAST + 1>(hp), acc);
    pairup(W[20], W[21]); dot8(W[20], W[21], dppu<BCAST + 2>(hp), acc);
    pairup(W[22], W[23]); dot8(W[22], W[23], dppu<BCAST + 3>(hp), acc);
    const f32x2 y = red_rows(acc);
    const int col = 128 * v + 8 * c + 2 * g, j = col / 10, part = (col - 10 * j) >> 1;
    __builtin_amdgcn_raw_buffer_store_b128(u32x4{tag, __float_as_uint(y[0]), tag, __float_as_uint(y[1])}, rs,
                                           WS_R2 + ((b * 64 + j) * 64 + pj) * R2_REC + part * 16, 0, 16);
  }
  {
    const rsrc_t rw = mkrs(p.wo, D * HD * 2);
    const int vo = ((h * DH + 32 * g) * D + 128 * v + 8 * c) * 2;
#pragma unroll
    for (int i = 0; i < 32; ++i) { W[i] = ldb<GM_WAUX>(rw, vo, i * (D * 2)); SB(); }
    SB();
#pragma unroll
    for (int k = 0; k < 16; ++k) { pairup(W[2 * k], W[2 * k + 1]); asm volatile("" : "+v"(W[2 * k]), "+v"(W[2 * k + 1])); }
  }
  float gate = 0.f;
  if (v == 0) {
    float sgm = 0.f;
#pragma unroll
    for (int i = 0; i < 5; ++i) {
      const unsigned xv = *(const GAS unsigned*)(p.x + b * D + 128 * i + 2 * lane), gv = *(const GAS unsigned*)(p.gw + 128 * i + 2 * lane);
      sgm += rbf(bflo(xv) * bflo(gv)) + rbf(bfhi(xv) * bfhi(gv));
    }
    sgm += dpp<XOR1>(sgm); sgm += dpp<XOR2>(sgm); sgm += dpp<XOR7>(sgm); sgm += dpp<ROR8>(sgm); sgm = xrow_sum(sgm);
    gate = rbf(1.f / (1.f + __expf(-rbf(sgm))));
    asm volatile("" : "+v"(gate));
    STAMP(3);
  }
  BAR();
  if (v == 0) STAMP(4);
  {
    const f32x4 mm = *(const f32x4*)&lds[L_M], ll = *(const f32x4*)&lds[L_L];
    const float M = fmaxf(fmaxf(mm[0], mm[1]), fmaxf(mm[2], mm[3]));
    f32x2 o = {0.f, 0.f};
    float L = 0.f;
#pragma unroll
    for (int w = 0; w < NSW; ++w) { const float wt = __builtin_amdgcn_exp2f(mm[w] - M); o += wt * *(const f32x2*)&lds[L_REC + w * 128 + 2 * lane]; L = fmaf(wt, ll[w], L); }
    const unsigned op = pk2(o[0], o[1]);
    float acc[8] = {0.f, 0.f, 0.f, 0.f, 0.f, 0.f, 0.f, 0.f};
    Gemv<0, 16, false>::run(W, op, acc);
    const f32x2 y = red_rows(acc);
    const int col = 128 * v + 8 * c + 2 * g, j = col / 10, part = (col - 10 * j) >> 1;
    __builtin_amdgcn_raw_buffer_store_b128(u32x4{tag, __float_as_uint(y[0]), tag, __float_as_uint(y[1])}, rs,
                                           WS_R1 + ((b * 64 + j) * 64 + pj) * R1_REC + part * 16, 0, 16);
    if (v == NSV - 1)
      __builtin_amdgcn_raw_buffer_store_b128(u32x4{tag, __float_as_uint(M), tag, __float_as_uint(L)}, rs,
                                             WS_R1 + ((b * 64 + lane) * 64 + pj) * R1_REC + 80, 0, 16);
  }
  if (v != 0) return;
  STAMP(5);
  {
    const int o1 = WS_R1 + (b * 64 + pj) * 64 * R1_REC + lane * 16, o2 = WS_R2 + (b * 64 + pj) * 64 * R2_REC + lane * 16;
    u32x4 u[11];
    for (unsigned spin = 0; spin < (1u << 22); ++spin) {
#pragma unroll
      for (int k = 0; k < 6; ++k) u[k] = __builtin_amdgcn_raw_buffer_load_b128(rs, o1 + k * 1024, 0, 16);
#pragma unroll
      for (int k = 0; k < 5; ++k) u[6 + k] = __builtin_amdgcn_raw_buffer_load_b128(rs, o2 + k * 1024, 0, 16);
      bool ok = true;
#pragma unroll
      for (int k = 0; k < 11; ++k) ok = ok && u[k][0] == tag && u[k][2] == tag;
      if (ok) break;
      __builtin_amdgcn_s_sleep(1);
    }
    STAMP(6);
    f32x2* gb = (f32x2*)&lds[L_G];
#pragma unroll
    for (int k = 0; k < 11; ++k) gb[k * 64 + lane] = f32x2{__uint_as_float(u[k][1]), __uint_as_float(u[k][3])};
    LGKM0();
    float y[16];
#pragma unroll
    for (int k = 0; k < 5; ++k) { const f32x2 t = gb[lane * 6 + k]; y[2 * k] = t[0]; y[2 * k + 1] = t[1]; }
    const f32x2 ml = gb[lane * 6 + 5];
    const float Mh = fmaxf(ml[0], dpp<XOR1>(ml[0]));
    const float wt = __builtin_amdgcn_exp2f(ml[0] - Mh);
    const float lw = wt * ml[1];
    const float coef = wt / (lw + dpp<XOR1>(lw));
    float z[16];
#pragma unroll
    for (int k = 0; k < 5; ++k) { const f32x2 t = gb[384 + lane * 5 + k]; z[2 * k] = t[0]; z[2 * k + 1] = t[1]; }
#pragma unroll
    for (int k = 0; k < 10; ++k) y[k] *= coef;
    float t10[10], t5[5];
#pragma unroll
    for (int k = 0; k < 10; ++k) t10[k] = tr32(y[k], z[k]);
#pragma unroll
    for (int k = 0; k < 5; ++k) { float t = tr16(t10[2 * k], t10[2 * k + 1]);
      t += dpp<XOR1>(t); t += dpp<XOR2>(t); t += dpp<XOR7>(t); t += dpp<ROR8>(t); t5[k] = t; }
    float sel = t5[0];
    sel = c == 1 ? t5[1] : sel; sel = c == 2 ? t5[2] : sel; sel = c == 3 ? t5[3] : sel; sel = c == 4 ? t5[4] : sel;
    sel = rbf(sel);
    sel = rbf(sel * (lane < 32 ? gate : rbf(1.f - gate)));
    const float tot = tr32(sel, sel);
    auto sw2 = __builtin_amdgcn_permlane16_swap(__float_as_uint(tot), __float_as_uint(tot), false, false);
    if (lane < 5) *(GAS unsigned*)(p.out + b * D + 10 * pj + 2 * lane) = pk2(__uint_as_float(sw2[0]), __uint_as_float(sw2[1]));
    if (pj == 0 && lane == 0) __hip_atomic_store((GAS unsigned*)(p.ws + WS_EPOCH + b * 256), tag, __ATOMIC_RELAXED, __HIP_MEMORY_SCOPE_AGENT);
    STAMP(7);
  }
}
}

extern "C" void kernel_launch(void* const* d_in, const int* in_sizes, int n_in, void* d_out, int out_size, void* d_ws, size_t ws_size,
                              hipStream_t stream) {
  gm::Params p;
  p.x = (const gm::u16*)d_in[0]; p.kc = (const gm::u16*)d_in[1]; p.vc = (const gm::u16*)d_in[2]; p.wq = (const gm::u16*)d_in[3];
  p.wo = (const gm::u16*)d_in[4]; p.wg = (const gm::u16*)d_in[6]; p.wu = (const gm::u16*)d_in[7]; p.wd = (const gm::u16*)d_in[8];
  p.gw = (const gm::u16*)d_in[9]; p.out = (gm::u16*)d_out; p.ws = (unsigned char*)d_ws;
  hipLaunchKernelGGL(gm::gm_fused, dim3(256), dim3(gm::NTHR), 0, stream, p);
}
```

```cpp
#include <hip/hip_runtime.h>
#include <hip/hip_bf16.h>
#include <stdint.h>

namespace gm {
constexpr int B = 4, D = 640, H = 32, SK = 4096, DH = 128, F = 1728, HD = H * DH;
constexpr int NSV = 4, NSW = 4, NTHR = 64 * (NSV + NSW);
#define GM_W0 10
#define GM_DELTA 1
#define GM_WAUX 2
#define GM_PACE 8
#define GM_PRIO 2
#define GM_RING 2
constexpr int W0 = GM_W0;
constexpr int RING = GM_RING;
constexpr int MAXP = 48;
constexpr int NP0 = 32 + GM_DELTA;

typedef unsigned u32x4 __attribute__((ext_vector_type(4)));
typedef float f32x4 __attribute__((ext_vector_type(4)));
typedef float f32x2 __attribute__((ext_vector_type(2)));
typedef __bf16 bf16x2 __attribute__((ext_vector_type(2)));
typedef unsigned short u16;
#define GAS __attribute__((address_space(1)))

constexpr int WS_EPOCH = 0;
constexpr int WS_Q = 4096;
constexpr int WS_FP = WS_Q + H * B * 8 * 1024;
constexpr int WS_H = WS_FP + H * 8 * 8 * 512;
constexpr int WS_LAM = WS_H + B * 8192;
constexpr int WS_G1 = WS_LAM + H * B * 128;
constexpr int WS_R = WS_G1 + H * B * 1024;
constexpr int WS_END = WS_R + B * 64 * 4096;
#define STAMP(i) do { } while (0)

constexpr int L_SY = 0;
constexpr int L_FQ = 12, L_FS = 13, L_FR = 14, L_TAG = 36;
constexpr int L_FK = 16;
constexpr int L_M = 20, L_L = 24, L_F = 28, L_GT = 32;
constexpr int L_QV = 48;
constexpr int L_REC = L_QV + 64;
constexpr int L_QP = L_REC + 512;
constexpr int L_FPP = L_QP + 2048;
constexpr int L_SCR = L_FPP + 1024;
constexpr int L_WO = L_SCR + 2048;
constexpr int L_WD = L_WO + 144 * 40;
constexpr int L_X = L_WD + 60 * 40;
constexpr int L_S = L_X + 1280;
constexpr int L_END0 = L_S + NSW * MAXP * 16;
constexpr int L_END = 21 * 1024;
static_assert(L_END0 <= L_END, "lds");

__device__ __forceinline__ float bflo(unsigned u) { return __builtin_bit_cast(float, u << 16); }
__device__ __forceinline__ float bfhi(unsigned u) { return __builtin_bit_cast(float, u & 0xffff0000u); }
__device__ __forceinline__ unsigned pk2(float lo, float hi) { bf16x2 v = {(__bf16)lo, (__bf16)hi}; return __builtin_bit_cast(unsigned, v); }
__device__ __forceinline__ float rbf(float f) { return bflo(pk2(f, f)); }
__device__ __forceinline__ float dot2(unsigned a, unsigned b, float acc) {
  return __builtin_amdgcn_fdot2_f32_bf16(__builtin_bit_cast(bf16x2, a), __builtin_bit_cast(bf16x2, b), acc, false);
}
template <int CTRL> __device__ __forceinline__ float dpp(float x) {
  return __builtin_bit_cast(float, __builtin_amdgcn_mov_dpp(__builtin_bit_cast(int, x), CTRL, 0xf, 0xf, true));
}
template <int CTRL> __device__ __forceinline__ unsigned dppu(unsigned x) {
  return (unsigned)__builtin_amdgcn_mov_dpp((int)x, CTRL, 0xf, 0xf, true);
}
constexpr int XOR1 = 0xB1, XOR2 = 0x4E, XOR7 = 0x141, ROR8 = 0x128, BCAST = 0x150, ROR4 = 0x124;
__device__ __forceinline__ float tr32(float a, float b) {
  auto t = __builtin_amdgcn_permlane32_swap(__float_as_uint(a), __float_as_uint(b), false, false);
  return __uint_as_float(t[0]) + __uint_as_float(t[1]);
}
__device__ __forceinline__ float tr16(float a, float b) {
  auto t = __builtin_amdgcn_permlane16_swap(__float_as_uint(a), __float_as_uint(b), false, false);
  return __uint_as_float(t[0]) + __uint_as_float(t[1]);
}
__device__ __forceinline__ float xrow_max(float x) {
  auto s = __builtin_amdgcn_permlane16_swap(__float_as_uint(x), __float_as_uint(x), false, false);
  x = fmaxf(__uint_as_float(s[0]), __uint_as_float(s[1]));
  auto t = __builtin_amdgcn_permlane32_swap(__float_as_uint(x), __float_as_uint(x), false, false);
  return fmaxf(__uint_as_float(t[0]), __uint_as_float(t[1]));
}
__device__ __forceinline__ float xrow_sum(float x) { x = tr16(x, x); return tr32(x, x); }
typedef __amdgpu_buffer_rsrc_t rsrc_t;
__device__ __forceinline__ rsrc_t mkrs(const void* p, int bytes) { return __builtin_amdgcn_make_buffer_rsrc((void*)p, 0, bytes, 0x00020000); }
template <int AUX> __device__ __forceinline__ u32x4 ldb(rsrc_t r, int voff, int soff) { return __builtin_amdgcn_raw_buffer_load_b128(r, voff, soff, AUX); }
__device__ __forceinline__ void pub(rsrc_t r, int off, unsigned tag, float a, float b) {
  __builtin_amdgcn_raw_buffer_store_b128(u32x4{tag, __float_as_uint(a), tag, __float_as_uint(b)}, r, off, 0, 16);
}
__device__ __forceinline__ unsigned plo(unsigned r0, unsigned r1) { return __builtin_amdgcn_perm(r1, r0, 0x05040100u); }
__device__ __forceinline__ unsigned phi(unsigned r0, unsigned r1) { return __builtin_amdgcn_perm(r1, r0, 0x07060302u); }
__device__ __forceinline__ void pairup(u32x4& r0, u32x4& r1) {
  u32x4 a, b;
  a[0] = plo(r0[0], r1[0]); a[1] = phi(r0[0], r1[0]); a[2] = plo(r0[1], r1[1]); a[3] = phi(r0[1], r1[1]);
  b[0] = plo(r0[2], r1[2]); b[1] = phi(r0[2], r1[2]); b[2] = plo(r0[3], r1[3]); b[3] = phi(r0[3], r1[3]);
  r0 = a; r1 = b;
}
__device__ __forceinline__ void dot8(const u32x4& a, const u32x4& b, unsigned xp, float (&acc)[8]) {
#pragma unroll
  for (int j = 0; j < 4; ++j) { acc[j] = dot2(a[j], xp, acc[j]); acc[4 + j] = dot2(b[j], xp, acc[4 + j]); }
}
__device__ __forceinline__ f32x2 red_rows(const float (&a)[8]) {
  const float r0 = tr32(a[0], a[4]), r1 = tr32(a[1], a[5]), r2 = tr32(a[2], a[6]), r3 = tr32(a[3], a[7]);
  return f32x2{tr16(r0, r2), tr16(r1, r3)};
}
#define LGKM0() asm volatile("s_waitcnt lgkmcnt(0)" ::: "memory")
#define BAR() do { LGKM0(); __builtin_amdgcn_s_barrier(); asm volatile("" ::: "memory"); } while (0)
#define SB() __builtin_amdgcn_sched_barrier(0)
#define WG_RLX __ATOMIC_RELAXED, __HIP_MEMORY_SCOPE_WORKGROUP
constexpr unsigned SPIN_CAP = 1u << 20;
__device__ __forceinline__ void lds_set(unsigned* flag, int lane) {
  LGKM0();
  if (lane == 0) __hip_atomic_store(flag, 1u, WG_RLX);
}
__device__ __forceinline__ void lds_wait(const unsigned* flag) {
  while (__hip_atomic_load(flag, WG_RLX) == 0u) __builtin_amdgcn_s_sleep(1);
  asm volatile("" ::: "memory");
}
__device__ __forceinline__ void lds_wait4(const unsigned* flag, int lane) {
  while (!__all(__hip_atomic_load(flag + (lane & 3), WG_RLX) != 0u)) __builtin_amdgcn_s_sleep(1);
  asm volatile("" ::: "memory");
}
template <int BB> __device__ __forceinline__ void proj5(const u32x4 (&W)[6], unsigned xw, float (&acc)[8]) {
  dot8(W[0], W[1], dppu<BCAST + 4 * BB + 0>(xw), acc);
  dot8(W[2], W[3], dppu<BCAST + 4 * BB + 1>(xw), acc);
  dot8(W[4], W[5], dppu<BCAST + 4 * BB + 2>(xw), acc);
}

struct Params {
  const u16 *x, *kc, *vc, *wq, *wo, *wg, *wu, *wd, *gw;
  u16* out;
  unsigned char* ws;
};

__global__ void __launch_bounds__(NTHR, 2) gm_fused(Params p) {
  __shared__ __attribute__((aligned(16))) float lds[L_END];
  unsigned* ldu = (unsigned*)lds;
  const int tid = threadIdx.x, lane = tid & 63, wv = __builtin_amdgcn_readfirstlane(tid >> 6);
  const int bid = blockIdx.x, xi = (bid & 7) >> 1, s = bid & 1, b = (bid >> 3) & 3, mm = bid >> 5;
  const int h = s ? (mm & 3) + 4 * xi + 16 * (mm >> 2) : xi + 4 * mm;
  const int u = 4 * s + b, pj = 2 * h + s;
  const int g = lane >> 4, c = lane & 15;
  const rsrc_t rs = mkrs(p.ws, WS_END);

  if (wv >= NSV) {
    const int sw = wv - NSV;
    const size_t base = (size_t)(b * H + h) * SK * DH;
    const rsrc_t rk = mkrs(p.kc + base, SK * DH * 2), rv = mkrs(p.vc + base, SK * DH * 2);
    const int lo = lane * 16;
    const int np = s ? 64 - NP0 : NP0;
    const int o0 = ((s ? NSW * NP0 : 0) + sw) * 4096;
    const int npe = W0 + (np - W0 + RING - 1) / RING * RING;
    u32x4 X[W0][4];
#define OOB 0x7f000000
#define LDX(d, rs_, pi) { const int off_ = (pi) < np ? o0 + (pi) * (NSW * 4096) : OOB; \
      _Pragma("unroll") for (int i = 0; i < 4; ++i) X[d][i] = ldb<2>(rs_, lo + i * 1024, off_); SB(); }
#define LDJ(d, j) { const int j_ = (j); const rsrc_t rj_ = j_ >= npe ? rv : rk; LDX(d, rj_, j_ >= npe ? j_ - npe : j_) }
    if (sw == 0) STAMP(0);
    LDX(0, rk, 0) LDX(1, rk, 1)
    BAR();
#define PACE() asm volatile("s_waitcnt vmcnt(%0)" :: "n"(GM_PACE - 4) : "memory")
#pragma unroll
    for (int d = 2; d < W0; ++d) { PACE(); LDX(d, rk, d) }
    lds_wait(ldu + L_FQ);
    const u32x4 q = *(const u32x4*)&ldu[L_QV + 4 * c];
    if (sw == 0) STAMP(1);
    float m = -INFINITY;
    const bool b8 = (c & 8) != 0, b4 = (c & 4) != 0;
    float* sp = &lds[L_S + sw * (MAXP * 16) + 4 * g];
#define KSTEP(d, t, COND, LOAD) { \
      float x_[4] = {0.f, 0.f, 0.f, 0.f}; \
      if (COND) { _Pragma("unroll") for (int i = 0; i < 4; ++i) { float t_ = dot2(X[d][i][0], q[0], 0.f); t_ = dot2(X[d][i][1], q[1], t_); t_ = dot2(X[d][i][2], q[2], t_); x_[i] = dot2(X[d][i][3], q[3], t_); } } \
      asm volatile("" : "+v"(x_[0]), "+v"(x_[1]), "+v"(x_[2]), "+v"(x_[3]) :: "memory"); SB(); LOAD \
      if (COND) { \
      const float y0_ = (b8 ? x_[2] : x_[0]) + dpp<ROR8>(b8 ? x_[0] : x_[2]); \
      const float y1_ = (b8 ? x_[3] : x_[1]) + dpp<ROR8>(b8 ? x_[1] : x_[3]); \
      float z_ = (b4 ? y1_ : y0_) + dpp<XOR7>(b4 ? y0_ : y1_); \
      z_ += dpp<XOR2>(z_); z_ += dpp<XOR1>(z_); \
      const float sc_ = rbf(rbf(z_) * 0.088388347648318440f) * 1.4426950408889634f; \
      m = fmaxf(m, sc_); \
      if ((c & 3) == 0) sp[(t) * 16 + (c >> 2)] = sc_; } SB(); }
#pragma unroll
    for (int d = 0; d < W0; ++d) { if (d < RING) KSTEP(d, d, true, LDJ(d, W0 + d)) else KSTEP(d, d, true, ) }
#pragma unroll 1
    for (int t = W0; t < npe; t += RING) {
#pragma unroll
      for (int d = 0; d < RING; ++d) KSTEP(d, t + d, t + d < np, LDJ(d, t + d + RING))
    }
    if (sw == 0) STAMP(11);
    m = fmaxf(m, dpp<ROR4>(m)); m = xrow_max(fmaxf(m, dpp<ROR8>(m)));
    float l = 0.f;
    asm volatile("" ::: "memory");
#pragma unroll
    for (int k = 0; k < MAXP / 16; ++k) {
      const int pc = c + 16 * k;
      if (pc < np) {
        f32x4 s4 = *(const f32x4*)&sp[pc * 16];
#pragma unroll
        for (int i = 0; i < 4; ++i) s4[i] = __builtin_amdgcn_exp2f(s4[i] - m);
        l += (s4[0] + s4[1]) + (s4[2] + s4[3]);
        *(f32x4*)&sp[pc * 16] = s4;
      }
    }
    asm volatile("" ::: "memory");
    l += dpp<XOR1>(l); l += dpp<XOR2>(l); l += dpp<XOR7>(l); l += dpp<ROR8>(l); l = xrow_sum(l);
    if (lane == 0) { lds[L_M + sw] = m; lds[L_L + sw] = l; }
    lds_set(ldu + L_FK + sw, lane);
    float acc[8] = {0.f, 0.f, 0.f, 0.f, 0.f, 0.f, 0.f, 0.f};
#define VSTEP(d, t, COND, LOAD) { \
      if (COND) { const f32x4 p4_ = *(const f32x4*)&sp[(t) * 16]; \
      _Pragma("unroll") for (int i = 0; i < 4; ++i) { _Pragma("unroll") for (int j = 0; j < 4; ++j) { \
        acc[2 * j] = fmaf(p4_[i], bflo(X[d][i][j]), acc[2 * j]); acc[2 * j + 1] = fmaf(p4_[i], bfhi(X[d][i][j]), acc[2 * j + 1]); } } } \
      asm volatile("" : "+v"(acc[0]), "+v"(acc[1]), "+v"(acc[2]), "+v"(acc[3]), "+v"(acc[4]), "+v"(acc[5]), "+v"(acc[6]), "+v"(acc[7]) :: "memory"); SB(); LOAD }
#pragma unroll 1
    for (int t = 0; t < np; t += RING) {
#pragma unroll
      for (int d = 0; d < RING; ++d) VSTEP(d, t + d, t + d < np, LDX(d, rv, t + d + RING))
    }
    if (sw == 0) STAMP(2);
    lds_wait(ldu + L_FS);
    *(f32x2*)&lds[L_REC + sw * 128 + 8 * c + 2 * g] = red_rows(acc) * lds[L_F + sw];
    BAR();
    if (sw != 0) return;
    const unsigned tag = ldu[L_TAG];
    const bool act = (lane & 7) < 5;
    const int ro = act ? WS_R + (b * 64 + pj) * 4096 + lane * 16 : OOB;
    lds_wait(ldu + L_FR);
    u32x4 r[4];
    _Pragma("unroll 1") for (unsigned spin = 0; spin < SPIN_CAP; ++spin) {
      asm volatile("" ::: "memory");
      bool ok = true;
#pragma unroll
      for (int k = 0; k < 4; ++k) r[k] = ldb<16>(rs, ro, k * 1024);
#pragma unroll
      for (int k = 0; k < 4; ++k) ok = ok && r[k][0] == tag && r[k][2] == tag;
      if (ok || !act) break;
      __builtin_amdgcn_s_sleep(1);
    }
    STAMP(6);
    float y0 = (__uint_as_float(r[0][1]) + __uint_as_float(r[1][1])) + (__uint_as_float(r[2][1]) + __uint_as_float(r[3][1]));
    float y1 = (__uint_as_float(r[0][3]) + __uint_as_float(r[1][3])) + (__uint_as_float(r[2][3]) + __uint_as_float(r[3][3]));
    y0 += dpp<ROR8>(y0); y1 += dpp<ROR8>(y1);
    y0 = xrow_sum(y0); y1 = xrow_sum(y1);
    if (lane < 5) *(GAS unsigned*)(p.out + b * D + 10 * pj + 2 * lane) = pk2(y0, y1);
    if (bid == 0 && lane == 0) __hip_atomic_store((GAS unsigned*)(p.ws + WS_EPOCH), tag, __ATOMIC_RELAXED, __HIP_MEMORY_SCOPE_AGENT);
    STAMP(7);
    return;
  }

  const int v = wv;
  __builtin_amdgcn_s_setprio(GM_PRIO);
  if (tid < 20) ldu[tid] = 0u;
  BAR();
  const unsigned ep = __hip_atomic_load((const GAS unsigned*)(p.ws + WS_EPOCH), __ATOMIC_RELAXED, __HIP_MEMORY_SCOPE_AGENT);
  const bool ffn = h < 27;
  u32x4 WQ[6], WF[6];
  unsigned xq;
  {
    const int k = c & 3, r0 = 80 * u + 20 * v;
    const int xr = k == 0 ? 2 * g : k == 1 ? 8 + 2 * g : 16 + (g & 2);
    xq = *(const GAS unsigned*)(p.x + (c >> 2) * D + r0 + xr);
    const rsrc_t rw = mkrs(p.wq, D * HD * 2);
    const int vo = ((r0 + 2 * g) * HD + h * DH + 8 * c) * 2;
    WQ[0] = ldb<GM_WAUX>(rw, vo, 0); WQ[1] = ldb<GM_WAUX>(rw, vo, HD * 2);
    WQ[2] = ldb<GM_WAUX>(rw, vo, 8 * HD * 2); WQ[3] = ldb<GM_WAUX>(rw, vo, 9 * HD * 2);
    WQ[4] = ldb<GM_WAUX>(rw, vo + (16 - g) * HD * 2, 0);
    SB();
    const int r8 = lane >> 3, f0 = 80 * u + 40 * (v >> 1);
    const rsrc_t rf = mkrs((v & 1) ? p.wu : p.wg, ffn ? D * F * 2 : 0);
    const int fo = ((f0 + 2 * r8) * F + 64 * h + 8 * (lane & 7)) * 2;
    WF[0] = ldb<GM_WAUX>(rf, fo, 0); WF[1] = ldb<GM_WAUX>(rf, fo, F * 2);
    WF[2] = ldb<GM_WAUX>(rf, fo, 16 * F * 2); WF[3] = ldb<GM_WAUX>(rf, fo, 17 * F * 2);
    WF[4] = ldb<GM_WAUX>(rf, fo + (32 - r8) * F * 2, 0);
    SB();
  }
  u32x4 xr0, xr1, gw0, gw1;
  {
    const rsrc_t rx = mkrs(p.x + v * D, D * 2), rgw = mkrs(p.gw, D * 2);
    xr0 = ldb<0>(rx, lane * 16, 0); xr1 = ldb<0>(rx, lane * 16, 1024);
    gw0 = ldb<0>(rgw, lane * 16, 0); gw1 = ldb<0>(rgw, lane * 16, 1024);
    SB();
  }
  {
    if ((c & 3) == 2) xq = (g & 1) ? xq >> 16 : xq & 0xffffu;
    WQ[5] = WQ[4];
    pairup(WQ[0], WQ[1]); pairup(WQ[2], WQ[3]); pairup(WQ[4], WQ[5]);
    float a0[8] = {}, a1[8] = {}, a2[8] = {}, a3[8] = {};
    proj5<0>(WQ, xq, a0); proj5<1>(WQ, xq, a1); proj5<2>(WQ, xq, a2); proj5<3>(WQ, xq, a3);
    float* qp = &lds[L_QP + v * 512 + 8 * c + 2 * g];
    *(f32x2*)(qp) = red_rows(a0); *(f32x2*)(qp + 128) = red_rows(a1); *(f32x2*)(qp + 256) = red_rows(a2); *(f32x2*)(qp + 384) = red_rows(a3);
  }
  *(u32x4*)&ldu[L_X + v * 320 + 4 * lane] = xr0;
  if (lane < 16) *(u32x4*)&ldu[L_X + v * 320 + 256 + 4 * lane] = xr1;
  const unsigned tag = ep * 0x9E3779B1u + 0x7F4A7C15u;
  lds_set(ldu + L_SY + v, lane); lds_wait4(ldu + L_SY, lane);
  {
    f32x2 t = *(const f32x2*)&lds[L_QP + v * 128 + 2 * lane];
#pragma unroll
    for (int w = 1; w < NSV; ++w) t += *(const f32x2*)&lds[L_QP + w * 512 + v * 128 + 2 * lane];
    pub(rs, WS_Q + ((h * B + v) * 8 + u) * 1024 + lane * 16, tag, t[0], t[1]);
  }
  if (v == 0) STAMP(3);
  if (ffn) {
    const int r8 = lane >> 3;
    const unsigned* xl = ldu + L_X + 40 * u + 20 * (v >> 1);
    WF[5] = WF[4];
    pairup(WF[0], WF[1]); pairup(WF[2], WF[3]); pairup(WF[4], WF[5]);
#pragma unroll
    for (int bb = 0; bb < 4; ++bb) {
      float a[8] = {};
      const unsigned x2 = xl[bb * 320 + 16 + (r8 >> 1)];
      dot8(WF[0], WF[1], xl[bb * 320 + r8], a); dot8(WF[2], WF[3], xl[bb * 320 + 8 + r8], a);
      dot8(WF[4], WF[5], (r8 & 1) ? x2 >> 16 : x2 & 0xffffu, a);
      f32x2 t = red_rows(a);
      t[0] += dpp<ROR8>(t[0]); t[1] += dpp<ROR8>(t[1]);
      if (c < 8) *(f32x2*)&lds[L_FPP + v * 256 + bb * 64 + 8 * c + 2 * g] = t;
    }
    lds_set(ldu + L_SY + 4 + v, lane); lds_wait4(ldu + L_SY + 4, lane);
    const int mat = v & 1, bb = 2 * (v >> 1) + (lane >> 5), cp = lane & 31;
    const f32x2 t = *(const f32x2*)&lds[L_FPP + mat * 256 + bb * 64 + 2 * cp] + *(const f32x2*)&lds[L_FPP + (mat + 2) * 256 + bb * 64 + 2 * cp];
    pub(rs, WS_FP + ((h * 8 + (cp >> 2)) * 8 + u) * 512 + (mat * 16 + bb * 4 + (cp & 3)) * 16, tag, t[0], t[1]);
  }
  if (v == 0) {
    const int qo = WS_Q + (h * B + b) * 8192 + lane * 16;
    u32x4 r[8];
    _Pragma("unroll 1") for (unsigned spin = 0; spin < SPIN_CAP; ++spin) {
      asm volatile("" ::: "memory");
      bool ok = true;
#pragma unroll
      for (int k = 0; k < 8; ++k) r[k] = ldb<16>(rs, qo + k * 1024, 0);
#pragma unroll
      for (int k = 0; k < 8; ++k) ok = ok && r[k][0] == tag && r[k][2] == tag;
      if (ok) break;
    }
    float q0 = 0.f, q1 = 0.f;
#pragma unroll
    for (int k = 0; k < 8; ++k) { q0 += __uint_as_float(r[k][1]); q1 += __uint_as_float(r[k][3]); }
    ldu[L_QV + lane] = pk2(q0, q1);
    if (lane == 0) ldu[L_TAG] = tag;
    lds_set(ldu + L_FQ, lane);
    STAMP(4);
  }
  {
    const rsrc_t rwo = mkrs(p.wo, HD * D * 2), rwd = mkrs(p.wd, F * D * 2);
    u32x4 t[5], t2[3];
#pragma unroll
    for (int i = 0; i < 5; ++i) { const int idx = (v * 5 + i) * 64 + lane, row = idx / 10, cg = idx - 10 * row;
      t[i] = ldb<GM_WAUX>(rwo, ((h * DH + row) * D + 80 * u + 8 * cg) * 2, 0); }
#pragma unroll
    for (int i = 0; i < 3; ++i) { const int idx = (v * 3 + i) * 64 + lane, row = idx / 10, cg = idx - 10 * row;
      t2[i] = ldb<GM_WAUX>(rwd, row < 54 ? ((54 * h + row) * D + 80 * u + 8 * cg) * 2 : 0x7f000000, 0); }
    SB();
    if (v == 1 && ffn) {
      const int fo = WS_FP + (h * 8 + u) * 4096 + lane * 16;
      u32x4 r[4];
      _Pragma("unroll 1") for (unsigned spin = 0; spin < SPIN_CAP; ++spin) {
      asm volatile("" ::: "memory");
        bool ok = true;
#pragma unroll
        for (int k = 0; k < 4; ++k) r[k] = ldb<16>(rs, fo + k * 1024, 0);
#pragma unroll
        for (int k = 0; k < 4; ++k) ok = ok && r[k][0] == tag && r[k][2] == tag;
        if (ok) break;
        __builtin_amdgcn_s_sleep(4);
      }
      float s0 = 0.f, s1 = 0.f;
#pragma unroll
      for (int k = 0; k < 4; ++k) { s0 += __uint_as_float(r[k][1]); s1 += __uint_as_float(r[k][3]); }
      s0 = rbf(tr32(s0, s0)); s1 = rbf(tr32(s1, s1));
      auto e0 = __builtin_amdgcn_permlane16_swap(__float_as_uint(s0), __float_as_uint(s0), false, false);
      auto e1 = __builtin_amdgcn_permlane16_swap(__float_as_uint(s1), __float_as_uint(s1), false, false);
      const unsigned hp = pk2(__uint_as_float(e0[0]) * __uint_as_float(e0[1]), __uint_as_float(e1[0]) * __uint_as_float(e1[1]));
      if (lane < 16)
        __hip_atomic_store((GAS unsigned long long*)(p.ws + WS_H + (lane >> 2) * 8192 + (32 * h + 4 * u + (lane & 3)) * 8),
                           ((unsigned long long)hp << 32) | tag, __ATOMIC_RELAXED, __HIP_MEMORY_SCOPE_AGENT);
    }
#pragma unroll
    for (int i = 0; i < 5; ++i) *(u32x4*)&ldu[L_WO + ((v * 5 + i) * 64 + lane) * 4] = t[i];
#pragma unroll
    for (int i = 0; i < 3; ++i) if ((v * 3 + i) * 64 + lane < 600) *(u32x4*)&ldu[L_WD + ((v * 3 + i) * 64 + lane) * 4] = t2[i];
    if (tid < 160) *(u32x4*)&ldu[L_WO + (1280 + tid) * 4] = u32x4{0u, 0u, 0u, 0u};
  }
  float gate;
  {
    float sgm = 0.f;
#pragma unroll
    for (int i = 0; i < 4; ++i) {
      sgm += rbf(bflo(xr0[i]) * bflo(gw0[i])) + rbf(bfhi(xr0[i]) * bfhi(gw0[i]));
      sgm += rbf(bflo(xr1[i]) * bflo(gw1[i])) + rbf(bfhi(xr1[i]) * bfhi(gw1[i]));
    }
    sgm += dpp<XOR1>(sgm); sgm += dpp<XOR2>(sgm); sgm += dpp<XOR7>(sgm); sgm += dpp<ROR8>(sgm); sgm = xrow_sum(sgm);
    gate = rbf(1.f / (1.f + __expf(-rbf(sgm))));
  }
  if (lane == 0) lds[L_GT + v] = gate;
  lds_set(ldu + L_SY + 8 + v, lane); lds_wait4(ldu + L_SY + 8, lane);
  const int g6 = min(lane / 10, 5), cg = lane - 10 * (lane / 10);
  unsigned* scr = ldu + L_SCR + v * 512;
  float acc[8] = {};
  {
    unsigned long long hv;
    const GAS unsigned long long* hp = (const GAS unsigned long long*)(p.ws + WS_H + v * 8192 + (27 * h + min(lane, 26)) * 8);
    _Pragma("unroll 1") for (unsigned spin = 0; spin < SPIN_CAP; ++spin) {
      asm volatile("" ::: "memory");
      hv = __hip_atomic_load(hp, __ATOMIC_RELAXED, __HIP_MEMORY_SCOPE_AGENT);
      if ((unsigned)hv == tag) break;
      __builtin_amdgcn_s_sleep(8);
    }
    scr[lane] = lane < 27 ? (unsigned)(hv >> 32) : 0u;
    LGKM0();
#pragma unroll
    for (int k = 0; k < 5; ++k) {
      u32x4 a = *(const u32x4*)&ldu[L_WD + (10 * g6 + 2 * k) * 40 + 4 * cg], bq = *(const u32x4*)&ldu[L_WD + (10 * g6 + 2 * k + 1) * 40 + 4 * cg];
      pairup(a, bq);
      dot8(a, bq, scr[5 * g6 + k], acc);
    }
    const float og = rbf(1.f - gate);
#pragma unroll
    for (int k = 0; k < 8; ++k) acc[k] *= og;
  }
  u32x4 W[24];
#pragma unroll
  for (int k = 0; k < 12; ++k) {
    W[2 * k] = *(const u32x4*)&ldu[L_WO + (24 * g6 + 2 * k) * 40 + 4 * cg]; W[2 * k + 1] = *(const u32x4*)&ldu[L_WO + (24 * g6 + 2 * k + 1) * 40 + 4 * cg];
    pairup(W[2 * k], W[2 * k + 1]);
    asm volatile("" : "+v"(W[2 * k]), "+v"(W[2 * k + 1]));
  }
  scr[64 + (lane & 7)] = 0u;
  asm volatile("" : "+v"(acc[0]), "+v"(acc[1]), "+v"(acc[2]), "+v"(acc[3]), "+v"(acc[4]), "+v"(acc[5]), "+v"(acc[6]), "+v"(acc[7]), "+v"(gate));
  if (v == 0) STAMP(5);
  if (v == 2) {
    lds_wait4(ldu + L_FK, lane);
    const f32x4 mm4 = *(const f32x4*)&lds[L_M], ll4 = *(const f32x4*)&lds[L_L];
    const float M = fmaxf(fmaxf(mm4[0], mm4[1]), fmaxf(mm4[2], mm4[3]));
    float L = 0.f;
#pragma unroll
    for (int w = 0; w < NSW; ++w) L = fmaf(__builtin_amdgcn_exp2f(mm4[w] - M), ll4[w], L);
    const int lo_ = WS_LAM + (h * B + b) * 128;
    if (lane == 0) pub(rs, lo_ + s * 64, tag, M, L);
    u32x4 r;
    _Pragma("unroll 1") for (unsigned spin = 0; spin < SPIN_CAP; ++spin) {
      asm volatile("" ::: "memory");
      r = ldb<16>(rs, lane == 0 ? lo_ + (1 - s) * 64 : 0x7f000000, 0);
      if (__builtin_amdgcn_readfirstlane(r[0]) == tag && __builtin_amdgcn_readfirstlane(r[2]) == tag) break;
      __builtin_amdgcn_s_sleep(8);
    }
    const float M2 = __uint_as_float(__builtin_amdgcn_readfirstlane(r[1])), L2 = __uint_as_float(__builtin_amdgcn_readfirstlane(r[3]));
    const float Mg = fmaxf(M, M2);
    const float e0 = L * __builtin_amdgcn_exp2f(M - Mg), e1 = L2 * __builtin_amdgcn_exp2f(M2 - Mg);
    const float Lg = s ? e1 + e0 : e0 + e1;
    if (lane < 4) lds[L_F + lane] = lds[L_GT + b] * __builtin_amdgcn_exp2f(lds[L_M + lane] - Mg) / Lg;
    lds_set(ldu + L_FS, lane);
  }
  BAR();
  if (v == 0) {
    STAMP(8);
    if (lane < 32) {
      const f32x4 o = (*(const f32x4*)&lds[L_REC + 4 * lane] + *(const f32x4*)&lds[L_REC + 128 + 4 * lane]) +
                      (*(const f32x4*)&lds[L_REC + 256 + 4 * lane] + *(const f32x4*)&lds[L_REC + 384 + 4 * lane]);
      __builtin_amdgcn_raw_buffer_store_b128(u32x4{tag, pk2(o[0], o[1]), tag, pk2(o[2], o[3])}, rs, WS_G1 + (h * B + b) * 1024 + s * 512 + lane * 16, 0, 16);
    }
  }
  {
    const int go = WS_G1 + (h * B + v) * 1024 + lane * 16;
    u32x4 r;
    _Pragma("unroll 1") for (unsigned spin = 0; spin < SPIN_CAP; ++spin) {
      asm volatile("" ::: "memory");
      r = ldb<16>(rs, go, 0);
      if (r[0] == tag && r[2] == tag) break;
      __builtin_amdgcn_s_sleep(1);
    }
    if (v == 0) STAMP(9);
    scr[2 * (lane & 31) + (lane >> 5)] = pk2(tr32(bflo(r[1]), bflo(r[3])), tr32(bfhi(r[1]), bfhi(r[3])));
    LGKM0();
    const u32x4 o0 = *(const u32x4*)&scr[12 * g6], o1 = *(const u32x4*)&scr[12 * g6 + 4], o2 = *(const u32x4*)&scr[12 * g6 + 8];
#pragma unroll
    for (int k = 0; k < 4; ++k) { dot8(W[2 * k], W[2 * k + 1], o0[k], acc); dot8(W[8 + 2 * k], W[9 + 2 * k], o1[k], acc); dot8(W[16 + 2 * k], W[17 + 2 * k], o2[k], acc); }
    float* sf = (float*)scr;
    if (lane < 60) { *(f32x4*)&sf[8 * lane] = f32x4{acc[0], acc[1], acc[2], acc[3]}; *(f32x4*)&sf[8 * lane + 4] = f32x4{acc[4], acc[5], acc[6], acc[7]}; }
    LGKM0();
    if (lane < 40) {
      f32x2 y = *(const f32x2*)&sf[2 * lane];
#pragma unroll
      for (int k = 1; k < 6; ++k) y += *(const f32x2*)&sf[80 * k + 2 * lane];
      const int jj = lane / 5, part = lane - 5 * jj;
      pub(rs, WS_R + ((v * 64 + 8 * u + jj) * 32 + h) * 128 + part * 16, tag, y[0], y[1]);
    }
    if (v == NSV - 1) lds_set(ldu + L_FR, lane);
    if (v == 0) STAMP(10);
  }
}
}

extern "C" void kernel_launch(void* const* d_in, const int* in_sizes, int n_in, void* d_out, int out_size, void* d_ws, size_t ws_size,
                              hipStream_t stream) {
  gm::Params p;
  p.x = (const gm::u16*)d_in[0]; p.kc = (const gm::u16*)d_in[1]; p.vc = (const gm::u16*)d_in[2]; p.wq = (const gm::u16*)d_in[3];
  p.wo = (const gm::u16*)d_in[4]; p.wg = (const gm::u16*)d_in[6]; p.wu = (const gm::u16*)d_in[7]; p.wd = (const gm::u16*)d_in[8];
  p.gw = (const gm::u16*)d_in[9]; p.out = (gm::u16*)d_out; p.ws = (unsigned char*)d_ws;
  hipLaunchKernelGGL(gm::gm_fused, dim3(256), dim3(gm::NTHR), 0, stream, p);
}
```

```cpp
#include <hip/hip_runtime.h>
#include <hip/hip_bf16.h>
#include <stdint.h>

namespace gm {
constexpr int B = 4, D = 640, H = 32, SK = 4096, DH = 128, F = 1728, HD = H * DH;
constexpr int NSV = 4, NSW = 4, NTHR = 64 * (NSV + NSW);
constexpr int NP = 32;
constexpr int NBUF = 6;
constexpr int T_A = 300, T_PIECE = 56, T_SHIFT = 6;
constexpr int T_Q = 400, T_F = 900, T_H = 1400, T_L = 600;
static_assert(NBUF % 2 == 0 && 2 * NBUF + 2 <= NP, "window");

typedef unsigned u32x4 __attribute__((ext_vector_type(4)));
typedef float f32x4 __attribute__((ext_vector_type(4)));
typedef float f32x2 __attribute__((ext_vector_type(2)));
typedef __bf16 bf16x2 __attribute__((ext_vector_type(2)));
typedef __bf16 bf16x8 __attribute__((ext_vector_type(8)));
typedef short s16x4 __attribute__((ext_vector_type(4)));
typedef unsigned u32x2 __attribute__((ext_vector_type(2)));
typedef unsigned short u16;
typedef __amdgpu_buffer_rsrc_t rsrc_t;
#define GAS __attribute__((address_space(1)))

constexpr int WS_EPOCH = 0;
constexpr int WS_Q = 4096;
constexpr int WS_FP = WS_Q + H * B * 4096;
constexpr int WS_H = WS_FP + H * 8 * 8 * 512;
constexpr int WS_LAM = WS_H + B * 8192;
constexpr int WS_G1 = WS_LAM + H * B * 128;
constexpr int WS_R = WS_G1 + H * B * 1024;
constexpr int WS_END = WS_R + B * 64 * 4096;
constexpr int OOB = 0x7f000000;

constexpr int L_SY = 0;
constexpr int L_FQ = 16, L_FS = 17, L_FR = 18;
constexpr int L_FK = 20;
constexpr int L_M = 24, L_L = 28, L_F = 32, L_GT = 36, L_TAG = 40;
constexpr int L_QV = 48;
constexpr int L_QP = L_QV + 64;
constexpr int L_SCR = L_QP;
constexpr int L_FPP = L_QP + 2048;
constexpr int L_REC = L_FPP;
constexpr int L_WO = L_FPP + 1024;
constexpr int L_WD = L_WO + 128 * 40;
constexpr int L_X = L_WD + 64 * 40;
constexpr int L_S = L_X + 1280;
constexpr int L_KB = L_S + NSW * NP * 16;
constexpr int L_END = L_KB + NSW * NBUF * 1024;

__device__ __forceinline__ float bflo(unsigned u) { return __builtin_bit_cast(float, u << 16); }
__device__ __forceinline__ float bfhi(unsigned u) { return __builtin_bit_cast(float, u & 0xffff0000u); }
__device__ __forceinline__ unsigned pk2(float lo, float hi) { bf16x2 v = {(__bf16)lo, (__bf16)hi}; return __builtin_bit_cast(unsigned, v); }
__device__ __forceinline__ float rbf(float f) { return bflo(pk2(f, f)); }
__device__ __forceinline__ float dot2(unsigned a, unsigned b, float acc) {
  return __builtin_amdgcn_fdot2_f32_bf16(__builtin_bit_cast(bf16x2, a), __builtin_bit_cast(bf16x2, b), acc, false);
}
template <int CTRL> __device__ __forceinline__ float dpp(float x) {
  return __builtin_bit_cast(float, __builtin_amdgcn_mov_dpp(__builtin_bit_cast(int, x), CTRL, 0xf, 0xf, true));
}
constexpr int XOR1 = 0xB1, XOR2 = 0x4E, XOR7 = 0x141, ROR8 = 0x128, ROR4 = 0x124;
__device__ __forceinline__ float tr32(float a, float b) {
  auto t = __builtin_amdgcn_permlane32_swap(__float_as_uint(a), __float_as_uint(b), false, false);
  return __uint_as_float(t[0]) + __uint_as_float(t[1]);
}
__device__ __forceinline__ float tr16(float a, float b) {
  auto t = __builtin_amdgcn_permlane16_swap(__float_as_uint(a), __float_as_uint(b), false, false);
  return __uint_as_float(t[0]) + __uint_as_float(t[1]);
}
__device__ __forceinline__ float xrow_max(float x) {
  auto s = __builtin_amdgcn_permlane16_swap(__float_as_uint(x), __float_as_uint(x), false, false);
  x = fmaxf(__uint_as_float(s[0]), __uint_as_float(s[1]));
  auto t = __builtin_amdgcn_permlane32_swap(__float_as_uint(x), __float_as_uint(x), false, false);
  return fmaxf(__uint_as_float(t[0]), __uint_as_float(t[1]));
}
__device__ __forceinline__ float xrow_sum(float x) { x = tr16(x, x); return tr32(x, x); }
__device__ __forceinline__ float row_sum(float x) { x += dpp<XOR1>(x); x += dpp<XOR2>(x); x += dpp<XOR7>(x); return x + dpp<ROR8>(x); }
__device__ __forceinline__ rsrc_t mkrs(const void* p, int bytes) { return __builtin_amdgcn_make_buffer_rsrc((void*)p, 0, bytes, 0x00020000); }
constexpr int AUX_DEF = 0, AUX_NT = 2, AUX_SC1 = 16;
template <int AUX> __device__ __forceinline__ u32x4 ldb(rsrc_t r, int voff, int soff) { return __builtin_amdgcn_raw_buffer_load_b128(r, voff, soff, AUX); }
__device__ __forceinline__ void pubu(rsrc_t r, int off, unsigned tag, unsigned a, unsigned b) {
  __builtin_amdgcn_raw_buffer_store_b128(u32x4{tag, a, tag, b}, r, off, 0, AUX_SC1);
}
__device__ __forceinline__ void pub(rsrc_t r, int off, unsigned tag, float a, float b) { pubu(r, off, tag, __float_as_uint(a), __float_as_uint(b)); }
__device__ __forceinline__ bool tagged(const u32x4& r, unsigned tag) { return r[0] == tag && r[2] == tag; }
__device__ __forceinline__ void pairup(u32x4& r0, u32x4& r1) {
  u32x4 a, b;
#pragma unroll
  for (int j = 0; j < 2; ++j) {
    a[2 * j] = __builtin_amdgcn_perm(r1[j], r0[j], 0x05040100u); a[2 * j + 1] = __builtin_amdgcn_perm(r1[j], r0[j], 0x07060302u);
    b[2 * j] = __builtin_amdgcn_perm(r1[2 + j], r0[2 + j], 0x05040100u); b[2 * j + 1] = __builtin_amdgcn_perm(r1[2 + j], r0[2 + j], 0x07060302u);
  }
  r0 = a; r1 = b;
}
__device__ __forceinline__ void dot8(const u32x4& a, const u32x4& b, unsigned xp, float (&acc)[8]) {
#pragma unroll
  for (int j = 0; j < 4; ++j) { acc[j] = dot2(a[j], xp, acc[j]); acc[4 + j] = dot2(b[j], xp, acc[4 + j]); }
}
__device__ __forceinline__ f32x2 red_rows(const float (&a)[8]) {
  const float r0 = tr32(a[0], a[4]), r1 = tr32(a[1], a[5]), r2 = tr32(a[2], a[6]), r3 = tr32(a[3], a[7]);
  return f32x2{tr16(r0, r2), tr16(r1, r3)};
}
#define LGKM0() asm volatile("s_waitcnt lgkmcnt(0)" ::: "memory")
#define BAR() do { LGKM0(); __builtin_amdgcn_s_barrier(); asm volatile("" ::: "memory"); } while (0)
#define SB() __builtin_amdgcn_sched_barrier(0)
#define WG_RLX __ATOMIC_RELAXED, __HIP_MEMORY_SCOPE_WORKGROUP
#define AG_RLX __ATOMIC_RELAXED, __HIP_MEMORY_SCOPE_AGENT
#define POLL _Pragma("unroll 1") for (unsigned spin_ = 0; spin_ < (1u << 20); ++spin_)
#define FRESH() asm volatile("" ::: "memory")
__device__ __forceinline__ unsigned now() { return (unsigned)__builtin_amdgcn_s_memrealtime(); }
__device__ __forceinline__ void until(unsigned t0, int ticks) { while ((int)(now() - t0) < ticks) __builtin_amdgcn_s_sleep(4); }
__device__ __forceinline__ void lds_set(unsigned* flag, int lane) {
  LGKM0();
  if (lane == 0) __hip_atomic_store(flag, 1u, WG_RLX);
}
__device__ __forceinline__ void lds_wait(const unsigned* flag) {
  while (__hip_atomic_load(flag, WG_RLX) == 0u) __builtin_amdgcn_s_sleep(1);
  asm volatile("" ::: "memory");
}
__device__ __forceinline__ void lds_wait4(const unsigned* flag, int lane) {
  while (!__all(__hip_atomic_load(flag + (lane & 3), WG_RLX) != 0u)) __builtin_amdgcn_s_sleep(1);
  asm volatile("" ::: "memory");
}
__device__ __forceinline__ void load40(u32x4 (&W)[6], rsrc_t r, int byte0, int stride, int lane) {
  const int r8 = lane >> 3, o = byte0 + 2 * r8 * stride + (lane & 7) * 16;
  W[0] = ldb<AUX_NT>(r, o, 0); W[1] = ldb<AUX_NT>(r, o, stride);
  W[2] = ldb<AUX_NT>(r, o, 16 * stride); W[3] = ldb<AUX_NT>(r, o, 17 * stride);
  W[4] = ldb<AUX_NT>(r, o + (32 - r8) * stride, 0);
  SB();
}
__device__ __forceinline__ void proj40(u32x4 (&W)[6], const unsigned* xl, float* dst, int lane) {
  const int r8 = lane >> 3;
  W[5] = W[4];
  pairup(W[0], W[1]); pairup(W[2], W[3]); pairup(W[4], W[5]);
#pragma unroll
  for (int bb = 0; bb < 4; ++bb) {
    float a[8] = {};
    const unsigned x2 = xl[bb * 320 + 16 + (r8 >> 1)];
    dot8(W[0], W[1], xl[bb * 320 + r8], a); dot8(W[2], W[3], xl[bb * 320 + 8 + r8], a);
    dot8(W[4], W[5], (r8 & 1) ? x2 >> 16 : x2 & 0xffffu, a);
    f32x2 t = red_rows(a);
    t[0] += dpp<ROR8>(t[0]); t[1] += dpp<ROR8>(t[1]);
    if ((lane & 15) < 8) *(f32x2*)&dst[bb * 64 + 8 * (lane & 15) + 2 * (lane >> 4)] = t;
  }
}

__device__ __forceinline__ u32x4 frag(const unsigned* w) {
  typedef __attribute__((address_space(3))) s16x4* lp;
  const u32x2 a = __builtin_bit_cast(u32x2, __builtin_amdgcn_ds_read_tr16_b64_v4i16((lp)w)), b = __builtin_bit_cast(u32x2, __builtin_amdgcn_ds_read_tr16_b64_v4i16((lp)(w + 160)));
  return u32x4{a[0], a[1], b[0], b[1]};
}
__device__ __forceinline__ f32x4 mfma(const u32x4& a, const u32x4& b, const f32x4& c) {
  return __builtin_amdgcn_mfma_f32_16x16x32_bf16(__builtin_bit_cast(bf16x8, a), __builtin_bit_cast(bf16x8, b), c, 0, 0, 0);
}

struct Params {
  const u16 *x, *kc, *vc, *wq, *wo, *wg, *wu, *wd, *gw;
  u16* out;
  unsigned char* ws;
};

__global__ void __launch_bounds__(NTHR, 2) gm_fused(Params p) {
  __shared__ __attribute__((aligned(16))) float lds[L_END];
  unsigned* ldu = (unsigned*)lds;
  const int tid = threadIdx.x, lane = tid & 63, wv = __builtin_amdgcn_readfirstlane(tid >> 6);
  const int bid = blockIdx.x, xi = (bid & 7) >> 1, s = bid & 1, b = (bid >> 3) & 3, mm = bid >> 5;
  const int h = s ? (mm & 3) + 4 * xi + 16 * (mm >> 2) : xi + 4 * mm;
  const int u = 4 * s + b, pj = 2 * h + s;
  const int g = lane >> 4, c = lane & 15;
  const rsrc_t rs = mkrs(p.ws, WS_END);
  const unsigned t0 = now();

  if (wv >= NSV) {
    const int sw = wv - NSV;
    const size_t base = ((size_t)(b * H + h) * SK + s * (SK / 2)) * DH;
    const rsrc_t rk = mkrs(p.kc + base, SK * DH), rv = mkrs(p.vc + base, SK * DH);
    const int lo = sw * 4096 + lane * 16;
    u32x4 X[2][4], q;
    int lvl = 1;
#define SCHED(n) lvl = ((int)(now() - t0) - (T_A + (n) * T_PIECE) + (2 << T_SHIFT)) >> T_SHIFT;
#define LADDER() asm volatile("s_waitcnt vmcnt(7)\n\ts_cmp_gt_i32 %0, 3\n\ts_cbranch_scc1 .Lgm%=\n\ts_waitcnt vmcnt(6)\n\ts_cmp_eq_u32 %0, 3\n\ts_cbranch_scc1 .Lgm%=\n\t" \
      "s_waitcnt vmcnt(5)\n\ts_cmp_eq_u32 %0, 2\n\ts_cbranch_scc1 .Lgm%=\n\ts_waitcnt vmcnt(4)\n\ts_cmp_eq_u32 %0, 1\n\ts_cbranch_scc1 .Lgm%=\n\ts_waitcnt vmcnt(3)\n.Lgm%=:" :: "s"(lvl) : "scc", "memory")
#define LD1(d, i, rs_, pi) { LADDER(); X[d][i] = ldb<AUX_NT>(rs_, lo + (i) * 1024, (pi) * (NSW * 4096)); SB(); }
#pragma unroll
    for (int d = 0; d < 2; ++d) {
#pragma unroll
      for (int i = 0; i < 4; ++i) { LD1(d, i, rk, d) if (d == 0 && i == 3) BAR();   }
    }
    u32x4* kb = (u32x4*)&ldu[L_KB + sw * (NBUF * 1024)] + lane;
    int nb = 0;
#pragma unroll 1
    while (nb < NBUF && __builtin_amdgcn_readfirstlane(__hip_atomic_load(ldu + L_FQ, WG_RLX)) == 0u) {
#pragma unroll
      for (int d = 0; d < 2; ++d) {
        SCHED(nb + d + 2)
#pragma unroll
        for (int i = 0; i < 4; ++i) { kb[((nb + d) * 4 + i) * 64] = X[d][i]; asm volatile("" ::: "memory"); SB(); LD1(d, i, rk, nb + d + 2) }
      }
      nb += 2;
    }
    lds_wait(ldu + L_FQ);
    q = *(const u32x4*)&ldu[L_QV + 4 * c];
    float m = -INFINITY, l = 0.f, acc[8] = {};
    const bool b8 = (c & 8) != 0, b4 = (c & 4) != 0;
    float* sp = &lds[L_S + sw * (NP * 16) + 4 * g];
#define QK1(K_, i) { float t_ = dot2(K_[0], q[0], 0.f); t_ = dot2(K_[1], q[1], t_); t_ = dot2(K_[2], q[2], t_); x_[i] = dot2(K_[3], q[3], t_); \
      asm volatile("" : "+v"(x_[i]) :: "memory"); SB(); }
#define SCORE(t) { \
      const float y0_ = (b8 ? x_[2] : x_[0]) + dpp<ROR8>(b8 ? x_[0] : x_[2]); \
      const float y1_ = (b8 ? x_[3] : x_[1]) + dpp<ROR8>(b8 ? x_[1] : x_[3]); \
      float z_ = (b4 ? y1_ : y0_) + dpp<XOR7>(b4 ? y0_ : y1_); \
      z_ += dpp<XOR2>(z_); z_ += dpp<XOR1>(z_); \
      const float sc_ = rbf(rbf(z_) * 0.088388347648318440f) * 1.4426950408889634f; \
      m = fmaxf(m, sc_); \
      if ((c & 3) == 0) sp[(t) * 16 + (c >> 2)] = sc_; SB(); }
#pragma unroll 1
    for (int t = nb; t < NP; t += 2) {
#pragma unroll
      for (int d = 0; d < 2; ++d) {
        const int j = t + d + 2;
        SCHED(j)
        const rsrc_t rj = j >= NP ? rv : rk;
        const int pi = j & (NP - 1);
        float x_[4];
#pragma unroll
        for (int i = 0; i < 4; ++i) { QK1(X[d][i], i) LD1(d, i, rj, pi) }
        SCORE(t + d)
      }
      if (nb > 0) {
        --nb;
        float x_[4];
#pragma unroll
        for (int i = 0; i < 4; ++i) { const u32x4 kk = kb[(nb * 4 + i) * 64]; QK1(kk, i) }
        SCORE(nb)
      }
    }
    m = fmaxf(m, dpp<ROR4>(m)); m = xrow_max(fmaxf(m, dpp<ROR8>(m)));
    asm volatile("" ::: "memory");
#pragma unroll
    for (int k = 0; k < NP / 16; ++k) {
      f32x4 s4 = *(const f32x4*)&sp[(c + 16 * k) * 16];
#pragma unroll
      for (int i = 0; i < 4; ++i) s4[i] = __builtin_amdgcn_exp2f(s4[i] - m);
      l += (s4[0] + s4[1]) + (s4[2] + s4[3]);
      *(f32x4*)&sp[(c + 16 * k) * 16] = s4;
    }
    asm volatile("" ::: "memory");
    l = xrow_sum(row_sum(l));
    if (lane == 0) { lds[L_M + sw] = m; lds[L_L + sw] = l; }
    lds_set(ldu + L_FK + sw, lane);
    float fs = 0.f;
#pragma unroll 1
    for (int t = 0; t < NP; t += 2) {
      if (t == NP - 4) { lds_wait(ldu + L_FS); fs = lds[L_F + sw]; }
#pragma unroll
      for (int d = 0; d < 2; ++d) {
        SCHED(NP + t + d + 2)
        if (t + 2 >= NP) lvl = 4;
        const f32x4 p4 = *(const f32x4*)&sp[(t + d) * 16];
#pragma unroll
        for (int i = 0; i < 4; ++i) {
#pragma unroll
          for (int j = 0; j < 4; ++j) { acc[2 * j] = fmaf(p4[i], bflo(X[d][i][j]), acc[2 * j]); acc[2 * j + 1] = fmaf(p4[i], bfhi(X[d][i][j]), acc[2 * j + 1]); }
          asm volatile("" : "+v"(acc[0]), "+v"(acc[1]), "+v"(acc[2]), "+v"(acc[3]), "+v"(acc[4]), "+v"(acc[5]), "+v"(acc[6]), "+v"(acc[7]) :: "memory"); SB();
          LD1(d, i, rv, t + d + 2)
        }
      }
    }
    *(f32x2*)&lds[L_REC + sw * 128 + 8 * c + 2 * g] = red_rows(acc) * fs;
    BAR();
    if (sw != 0) return;
    const unsigned tag = ldu[L_TAG];
    const bool act = (lane & 7) < 5;
    const int ro = act ? WS_R + (b * 64 + pj) * 4096 + lane * 16 : OOB;
    lds_wait(ldu + L_FR);
    __builtin_amdgcn_s_sleep(10);
    u32x4 r[4];
    POLL {
      FRESH();
#pragma unroll
      for (int k = 0; k < 4; ++k) r[k] = ldb<AUX_SC1>(rs, ro, k * 1024);
      if (!act || (tagged(r[0], tag) && tagged(r[1], tag) && tagged(r[2], tag) && tagged(r[3], tag))) break;
      __builtin_amdgcn_s_sleep(1);
    }
    float y0 = (__uint_as_float(r[0][1]) + __uint_as_float(r[1][1])) + (__uint_as_float(r[2][1]) + __uint_as_float(r[3][1]));
    float y1 = (__uint_as_float(r[0][3]) + __uint_as_float(r[1][3])) + (__uint_as_float(r[2][3]) + __uint_as_float(r[3][3]));
    y0 = xrow_sum(y0 + dpp<ROR8>(y0)); y1 = xrow_sum(y1 + dpp<ROR8>(y1));
    if (lane < 5) *(GAS unsigned*)(p.out + b * D + 10 * pj + 2 * lane) = pk2(y0, y1);
    if (bid == 0 && lane == 0) __hip_atomic_store((GAS unsigned*)(p.ws + WS_EPOCH), tag, AG_RLX);
    return;
  }

  const int v = wv;
  __builtin_amdgcn_s_setprio(2);
  if (tid < 24) ldu[tid] = 0u;
  BAR();
  const unsigned ep = __hip_atomic_load((const GAS unsigned*)(p.ws + WS_EPOCH), AG_RLX);
  SB();
  const bool ffn = h < 27;
  u32x4 WQ[6], WF[6], xr0, xr1, gw0, gw1;
  {
    const rsrc_t rx = mkrs(p.x + v * D, D * 2), rgw = mkrs(p.gw, D * 2);
    xr0 = ldb<AUX_DEF>(rx, lane * 16, 0); xr1 = ldb<AUX_DEF>(rx, lane * 16, 1024);
    SB();
    load40(WQ, mkrs(p.wq, D * HD * 2), ((160 * (u >> 1) + 40 * v) * HD + DH * h + 64 * (u & 1)) * 2, HD * 2, lane);
    load40(WF, mkrs((v & 1) ? p.wu : p.wg, ffn ? D * F * 2 : 0), ((80 * u + 40 * (v >> 1)) * F + 64 * h) * 2, F * 2, lane);
    gw0 = ldb<AUX_DEF>(rgw, lane * 16, 0); gw1 = ldb<AUX_DEF>(rgw, lane * 16, 1024);
    SB();
  }
  *(u32x4*)&ldu[L_X + v * 320 + 4 * lane] = xr0;
  if (lane < 16) *(u32x4*)&ldu[L_X + v * 320 + 256 + 4 * lane] = xr1;
  lds_set(ldu + L_SY + v, lane); lds_wait4(ldu + L_SY, lane);
  const unsigned tag = ep * 0x9E3779B1u + 0x7F4A7C15u;
  proj40(WQ, ldu + L_X + 80 * (u >> 1) + 20 * v, &lds[L_QP + v * 256], lane);
  lds_set(ldu + L_SY + 4 + v, lane); lds_wait4(ldu + L_SY + 4, lane);
  if (lane < 32) {
    const float* qp = &lds[L_QP + v * 64 + 2 * lane];
    const f32x2 t = (*(const f32x2*)qp + *(const f32x2*)(qp + 256)) + (*(const f32x2*)(qp + 512) + *(const f32x2*)(qp + 768));
    pub(rs, WS_Q + (h * B + v) * 4096 + ((u & 1) * 4 + (u >> 1)) * 512 + lane * 16, tag, t[0], t[1]);
  }
  if (ffn) {
    proj40(WF, ldu + L_X + 40 * u + 20 * (v >> 1), &lds[L_FPP + v * 256], lane);
    lds_set(ldu + L_SY + 8 + v, lane); lds_wait4(ldu + L_SY + 8, lane);
    const int mat = v & 1, bb = 2 * (v >> 1) + (lane >> 5), cp = lane & 31;
    const f32x2 t = *(const f32x2*)&lds[L_FPP + mat * 256 + bb * 64 + 2 * cp] + *(const f32x2*)&lds[L_FPP + (mat + 2) * 256 + bb * 64 + 2 * cp];
    pub(rs, WS_FP + ((h * 8 + (cp >> 2)) * 8 + u) * 512 + (mat * 16 + bb * 4 + (cp & 3)) * 16, tag, t[0], t[1]);
  }
  if (v == 0) {
    const int qo = WS_Q + (h * B + b) * 4096 + lane * 16;
    until(t0, T_Q);
    u32x4 r[4];
    POLL {
      FRESH();
#pragma unroll
      for (int k = 0; k < 4; ++k) r[k] = ldb<AUX_SC1>(rs, qo, k * 1024);
      if (tagged(r[0], tag) && tagged(r[1], tag) && tagged(r[2], tag) && tagged(r[3], tag)) break;
    }
    ldu[L_QV + lane] = pk2(tr32(__uint_as_float(r[0][1]) + __uint_as_float(r[1][1]), __uint_as_float(r[2][1]) + __uint_as_float(r[3][1])),
                           tr32(__uint_as_float(r[0][3]) + __uint_as_float(r[1][3]), __uint_as_float(r[2][3]) + __uint_as_float(r[3][3])));
    if (lane == 0) ldu[L_TAG] = tag;
    lds_set(ldu + L_FQ, lane);
  }
  {
    const rsrc_t rwo = mkrs(p.wo, HD * D * 2), rwd = mkrs(p.wd, F * D * 2);
    u32x4 t[5], t2[3];
#pragma unroll
    for (int i = 0; i < 5; ++i) { const int idx = (v * 5 + i) * 64 + lane, row = idx / 10, cg = idx - 10 * row;
      t[i] = ldb<AUX_NT>(rwo, ((h * DH + row) * D + 80 * u + 8 * cg) * 2, 0); }
#pragma unroll
    for (int i = 0; i < 3; ++i) { const int idx = (v * 3 + i) * 64 + lane, row = idx / 10, cg = idx - 10 * row;
      t2[i] = ldb<AUX_NT>(rwd, row < 54 ? ((54 * h + row) * D + 80 * u + 8 * cg) * 2 : OOB, 0); }
    SB();
    if (v == 1 && ffn) {
      const int fo = WS_FP + (h * 8 + u) * 4096 + lane * 16;
      until(t0, T_F);
      u32x4 r[4];
      POLL {
        FRESH();
#pragma unroll
        for (int k = 0; k < 4; ++k) r[k] = ldb<AUX_SC1>(rs, fo, k * 1024);
        if (tagged(r[0], tag) && tagged(r[1], tag) && tagged(r[2], tag) && tagged(r[3], tag)) break;
        __builtin_amdgcn_s_sleep(4);
      }
      float s0 = (__uint_as_float(r[0][1]) + __uint_as_float(r[1][1])) + (__uint_as_float(r[2][1]) + __uint_as_float(r[3][1]));
      float s1 = (__uint_as_float(r[0][3]) + __uint_as_float(r[1][3])) + (__uint_as_float(r[2][3]) + __uint_as_float(r[3][3]));
      s0 = rbf(tr32(s0, s0)); s1 = rbf(tr32(s1, s1));
      auto e0 = __builtin_amdgcn_permlane16_swap(__float_as_uint(s0), __float_as_uint(s0), false, false);
      auto e1 = __builtin_amdgcn_permlane16_swap(__float_as_uint(s1), __float_as_uint(s1), false, false);
      const unsigned hp = pk2(__uint_as_float(e0[0]) * __uint_as_float(e0[1]), __uint_as_float(e1[0]) * __uint_as_float(e1[1]));
      if (lane < 16)
        __hip_atomic_store((GAS unsigned long long*)(p.ws + WS_H + (lane >> 2) * 8192 + (32 * h + 4 * u + (lane & 3)) * 8),
                           ((unsigned long long)hp << 32) | tag, AG_RLX);
    }
#pragma unroll
    for (int i = 0; i < 5; ++i) *(u32x4*)&ldu[L_WO + ((v * 5 + i) * 64 + lane) * 4] = t[i];
#pragma unroll
    for (int i = 0; i < 3; ++i) if ((v * 3 + i) * 64 + lane < 640) *(u32x4*)&ldu[L_WD + ((v * 3 + i) * 64 + lane) * 4] = t2[i];
  }
  float gate;
  {
    float sg = 0.f;
#pragma unroll
    for (int i = 0; i < 4; ++i) {
      sg += rbf(bflo(xr0[i]) * bflo(gw0[i])) + rbf(bfhi(xr0[i]) * bfhi(gw0[i]));
      sg += rbf(bflo(xr1[i]) * bflo(gw1[i])) + rbf(bfhi(xr1[i]) * bfhi(gw1[i]));
    }
    gate = rbf(1.f / (1.f + __expf(-rbf(xrow_sum(row_sum(sg))))));
  }
  if (lane == 0) lds[L_GT + v] = gate;
  lds_set(ldu + L_SY + 12 + v, lane); lds_wait4(ldu + L_SY + 12, lane);
  const int fw = (8 * g + (c >> 2)) * 40 + 2 * (c & 3);
  unsigned* scr = ldu + L_SCR + v * 512;
  f32x4 acc[5] = {};
  {
    unsigned long long hv = 0ull;
    const GAS unsigned long long* hp = (const GAS unsigned long long*)(p.ws + WS_H + v * 8192 + (27 * h + min(lane, 26)) * 8);
    until(t0, T_H);
    POLL {
      hv = __hip_atomic_load(hp, AG_RLX);
      if ((unsigned)hv == tag) break;
      __builtin_amdgcn_s_sleep(8);
    }
    scr[lane] = lane < 27 ? (unsigned)(hv >> 32) : 0u;
    LGKM0();
#pragma unroll
    for (int ks = 0; ks < 2; ++ks) {
      const u32x4 a = *(const u32x4*)&scr[16 * ks + 4 * g];
#pragma unroll
      for (int t = 0; t < 5; ++t) acc[t] = mfma(a, frag(ldu + L_WD + fw + ks * 1280 + t * 8), acc[t]);
    }
    const float og = rbf(1.f - gate);
#pragma unroll
    for (int t = 0; t < 5; ++t) acc[t] *= og;
  }
  u32x4 W[4][5];
#pragma unroll
  for (int ks = 0; ks < 4; ++ks)
#pragma unroll
    for (int t = 0; t < 5; ++t) { W[ks][t] = frag(ldu + L_WO + fw + ks * 1280 + t * 8); asm volatile("" : "+v"(W[ks][t])); }
  asm volatile("" : "+v"(acc[0]), "+v"(acc[1]), "+v"(acc[2]), "+v"(acc[3]), "+v"(acc[4]));
  const int pcol = (c & 1) ? 63 + c : 16 * g + c, pjj = pcol / 10;
  int po = ((c & 1) && g) ? OOB : WS_R + ((v * 64 + 8 * u + pjj) * 32 + h) * 128 + (pcol - 10 * pjj) * 8;
  asm volatile("" : "+v"(po));
  const int go = WS_G1 + (h * B + v) * 1024 + lane * 16;
  {
    const u32x4 w0 = ldb<AUX_SC1>(rs, go, 0), w1 = ldb<AUX_SC1>(rs, (lane & 7) < 5 ? WS_R + (b * 64 + pj) * 4096 + v * 1024 + lane * 16 : OOB, 0);
    asm volatile("" :: "v"(w0), "v"(w1));
  }
  if (v == 2) {
    lds_wait4(ldu + L_FK, lane);
    const f32x4 mm4 = *(const f32x4*)&lds[L_M], ll4 = *(const f32x4*)&lds[L_L];
    const float M = fmaxf(fmaxf(mm4[0], mm4[1]), fmaxf(mm4[2], mm4[3]));
    float L = 0.f;
#pragma unroll
    for (int w = 0; w < NSW; ++w) L = fmaf(__builtin_amdgcn_exp2f(mm4[w] - M), ll4[w], L);
    const int lam = WS_LAM + (h * B + b) * 128;
    pub(rs, lane == 0 ? lam + s * 64 : OOB, tag, M, L);
    until(now(), T_L);
    u32x4 r;
    POLL {
      FRESH();
      r = ldb<AUX_SC1>(rs, lane == 0 ? lam + (1 - s) * 64 : OOB, 0);
      if (__builtin_amdgcn_readfirstlane(r[0]) == tag && __builtin_amdgcn_readfirstlane(r[2]) == tag) break;
      __builtin_amdgcn_s_sleep(8);
    }
    const float M2 = __uint_as_float(__builtin_amdgcn_readfirstlane(r[1])), L2 = __uint_as_float(__builtin_amdgcn_readfirstlane(r[3]));
    const float Mg = fmaxf(M, M2);
    const float Lg = L * __builtin_amdgcn_exp2f(M - Mg) + L2 * __builtin_amdgcn_exp2f(M2 - Mg);
    if (lane < 4) lds[L_F + lane] = lds[L_GT + b] * __builtin_amdgcn_exp2f(lds[L_M + lane] - Mg) / Lg;
    lds_set(ldu + L_FS, lane);
  }
  BAR();
  if (v == 0 && lane < 32) {
    const f32x4 o = (*(const f32x4*)&lds[L_REC + 4 * lane] + *(const f32x4*)&lds[L_REC + 128 + 4 * lane]) +
                    (*(const f32x4*)&lds[L_REC + 256 + 4 * lane] + *(const f32x4*)&lds[L_REC + 384 + 4 * lane]);
    pubu(rs, WS_G1 + (h * B + b) * 1024 + s * 512 + lane * 16, tag, pk2(o[0], o[1]), pk2(o[2], o[3]));
  }
  {
    u32x4 r;
    POLL {
      FRESH();
      r = ldb<AUX_SC1>(rs, go, 0);
      if (tagged(r, tag)) break;
      __builtin_amdgcn_s_sleep(1);
    }
    scr[2 * (lane & 31) + (lane >> 5)] = pk2(tr32(bflo(r[1]), bflo(r[3])), tr32(bfhi(r[1]), bfhi(r[3])));
    LGKM0();
#pragma unroll
    for (int ks = 0; ks < 4; ++ks) {
      const u32x4 a = *(const u32x4*)&scr[16 * ks + 4 * g];
#pragma unroll
      for (int t = 0; t < 5; ++t) acc[t] = mfma(a, W[ks][t], acc[t]);
    }
    const float e01 = (lane & 16) ? acc[1][0] : acc[0][0], e23 = (lane & 16) ? acc[3][0] : acc[2][0], e = (lane & 32) ? e23 : e01;
    const float ex = dpp<XOR1>(e), f4 = acc[4][0], fx = dpp<XOR1>(f4);
    pub(rs, po, tag, (c & 1) ? fx : e, (c & 1) ? f4 : ex);
    if (v == NSV - 1) lds_set(ldu + L_FR, lane);
  }
}
}

extern "C" void kernel_launch(void* const* d_in, const int* in_sizes, int n_in, void* d_out, int out_size, void* d_ws, size_t ws_size,
                              hipStream_t stream) {
  gm::Params p;
  p.x = (const gm::u16*)d_in[0]; p.kc = (const gm::u16*)d_in[1]; p.vc = (const gm::u16*)d_in[2]; p.wq = (const gm::u16*)d_in[3];
  p.wo = (const gm::u16*)d_in[4]; p.wg = (const gm::u16*)d_in[6]; p.wu = (const gm::u16*)d_in[7]; p.wd = (const gm::u16*)d_in[8];
  p.gw = (const gm::u16*)d_in[9]; p.out = (gm::u16*)d_out; p.ws = (unsigned char*)d_ws;
  hipLaunchKernelGGL(gm::gm_fused, dim3(256), dim3(gm::NTHR), 0, stream, p);
}
```
